```python
import torch

def _softmax(x, axis=-1):
    return torch.softmax(x, dim=axis)
def _gelu(x):
    return torch.nn.functional.gelu(x, approximate="tanh")
def _silu(x):
    return torch.nn.functional.silu(x)
def _sigmoid(x):
    return torch.sigmoid(x)
def _layernorm(x, axis=-1, eps=1e-5):
    m = x.mean(dim=axis, keepdim=True)
    v = x.var(dim=axis, keepdim=True, unbiased=False)
    return (x - m) / torch.sqrt(v + eps)
def _scatter_add(tgt, idx, src, axis=0):
    return tgt.clone().index_add_(axis, idx, src)
def _segment_sum(src, seg, n):
    out = torch.zeros((n,) + src.shape[1:], dtype=src.dtype)
    return out.index_add_(0, seg, src)
def _pad(x, pw):
    flat = tuple(v for pair in reversed(pw) for v in pair)
    return torch.nn.functional.pad(x, flat)

kernel_name = 'concat_heads__softmax_full_mha__spectral_fnet__prompt'

def setup_inputs():

    _w = (lambda a: (lambda t: t.to(torch.bfloat16) if t.is_floating_point() else t)(torch.as_tensor(a)))
    def _A_setup():
        B, S, D = 1, 2048, 448
        return {
            "x": _w((lambda *s: torch.randn(*s, dtype=torch.bfloat16) * 0.2)(B, S, D)),
            "wqkv": _w((lambda *s: torch.randn(*s, dtype=torch.bfloat16) * 0.8)(D, 3 * D)),
            "wo": _w((lambda *s: torch.randn(*s, dtype=torch.bfloat16) * 0.2)(D, D)),
        }
    _A_in = _A_setup()

    def _B_setup():
        B, S, D = 1, 2048, 448
        ns = torch.arange(S)[:, None] * torch.arange(S)[None, :]
        nd = torch.arange(D)[:, None] * torch.arange(D)[None, :]
        Ws_re = torch.cos(-2.0 * torch.pi * ns / S).to(float)
        Ws_im = torch.sin(-2.0 * torch.pi * ns / S).to(float)
        Wd_re = torch.cos(-2.0 * torch.pi * nd / D).to(float)
        Wd_im = torch.sin(-2.0 * torch.pi * nd / D).to(float)
        return {
            "x": _w((lambda *s: torch.randn(*s, dtype=torch.bfloat16) * 0.05)(B, S, D)),
            "dft_seq_re": _w(Ws_re),
            "dft_seq_im": _w(Ws_im),
            "dft_hid_re": _w(Wd_re),
            "dft_hid_im": _w(Wd_im),
        }
    _B_in = _B_setup()

    return {
        "A_x": _A_in["x"],
        "A_wqkv": _A_in["wqkv"],
        "A_wo": _A_in["wo"],
        "B_x": _B_in["x"],
        "B_dft_seq_re": _B_in["dft_seq_re"],
        "B_dft_seq_im": _B_in["dft_seq_im"],
        "B_dft_hid_re": _B_in["dft_hid_re"],
        "B_dft_hid_im": _B_in["dft_hid_im"],
    }

def reference(A_x, A_wqkv, A_wo, B_x, B_dft_seq_re, B_dft_seq_im, B_dft_hid_re, B_dft_hid_im):
    def _A_ref(x, wqkv, wo):
        B, S, D = x.shape
        H = 16; Dh = D // H
        qkv = (lambda x, *d: x.permute(*d))((x @ wqkv).reshape(B, S, 3, H, Dh), 2, 0, 3, 1, 4)
        q, k, v = qkv[0], qkv[1], qkv[2]
        scale = Dh ** -0.5
        scores = torch.einsum("bhqd,bhkd->bhqk", q, k) * scale
        p = _softmax(scores, axis=-1)
        o = torch.einsum("bhqk,bhkd->bhqd", p, v)
        return (lambda x, *d: x.permute(*d))(o, 0, 2, 1, 3).reshape(B, S, D) @ wo

    def _B_ref(x, dft_seq_re, dft_seq_im, dft_hid_re, dft_hid_im):
        a = torch.einsum("bsd,ed->bse", x, dft_hid_re)
        b = torch.einsum("bsd,ed->bse", x, dft_hid_im)
        y = torch.einsum("ts,bse->bte", dft_seq_re, a) - torch.einsum("ts,bse->bte", dft_seq_im, b)
        return y

    a = _A_ref(A_x, A_wqkv, A_wo)
    b = _B_ref(B_x, B_dft_seq_re, B_dft_seq_im, B_dft_hid_re, B_dft_hid_im)
    return torch.concatenate([a, b], axis=-1)
```

```cpp
#include <hip/hip_runtime.h>
#include <stdint.h>

typedef __attribute__((ext_vector_type(8))) short bf16x8;
typedef __attribute__((ext_vector_type(4))) short s16x4;
typedef __attribute__((ext_vector_type(16))) float f32x16;
typedef __attribute__((ext_vector_type(4))) unsigned u32x4;
typedef __attribute__((ext_vector_type(2))) unsigned u32x2;
typedef __attribute__((ext_vector_type(2))) float f32x2;
typedef __attribute__((ext_vector_type(4))) float f32x4;
typedef __attribute__((ext_vector_type(2))) __bf16 bf16x2_t;
typedef unsigned short u16;
typedef __attribute__((address_space(3))) const char* lds_cptr;
typedef short v4i16_t __attribute__((ext_vector_type(4)));

#define MFMA(a, b, c) __builtin_amdgcn_mfma_f32_32x32x16_bf16(a, b, c, 0, 0, 0)
#define ATT_NT 4
#define GEMM_N 7
#define SBAR() __builtin_amdgcn_sched_barrier(0)

constexpr int SEQ = 2048, DM = 448;
#define STAMP(k, i)
constexpr size_t WS_QF = 0;
constexpr size_t WS_KVF = WS_QF + 2097152;
constexpr size_t WS_ABF = WS_KVF + 4194304;
constexpr size_t WS_WOF = WS_ABF + 3670016;
constexpr size_t WS_OF = WS_WOF + 458752;
constexpr size_t WS_YP = WS_OF + 2097152;

__device__ __forceinline__ unsigned cvtpk(float lo, float hi) {
    f32x2 v = {lo, hi};
    return __builtin_bit_cast(unsigned, __builtin_convertvector(v, bf16x2_t));
}
__device__ __forceinline__ void glds(unsigned voff, const void* sbase, unsigned lds_base) {
    asm volatile("s_mov_b32 m0, %2\n\ts_nop 0\n\tglobal_load_lds_dwordx4 %0, %1" ::"v"(voff), "s"(sbase), "s"(lds_base) : "memory");
}
__device__ __forceinline__ void glds4(unsigned voff, const void* sbase, unsigned lds_base) {
    asm volatile("s_mov_b32 m0, %2\n\ts_nop 0\n\tglobal_load_lds_dwordx4 %0, %1\n\tglobal_load_lds_dwordx4 %0, %1 offset:1024\n\t"
                 "global_load_lds_dwordx4 %0, %1 offset:2048\n\tglobal_load_lds_dwordx4 %0, %1 offset:3072"
                 ::"v"(voff), "s"(sbase), "s"(lds_base) : "memory");
}
__device__ __forceinline__ void glds3(unsigned voff, const void* sbase, unsigned lds_base) {
    asm volatile("s_mov_b32 m0, %2\n\ts_nop 0\n\tglobal_load_lds_dwordx4 %0, %1\n\tglobal_load_lds_dwordx4 %0, %1 offset:1024\n\t"
                 "global_load_lds_dwordx4 %0, %1 offset:2048"
                 ::"v"(voff), "s"(sbase), "s"(lds_base) : "memory");
}
__device__ __forceinline__ void gload16(bf16x8& d, const void* g) {
    asm volatile("global_load_dwordx4 %0, %1, off" : "=v"(d) : "v"(g) : "memory");
}
__device__ __forceinline__ bf16x8 ldsr128(lds_cptr p) { return *(const __attribute__((address_space(3))) bf16x8*)p; }
__device__ __forceinline__ s16x4 vtr(lds_cptr p) {
    return __builtin_bit_cast(s16x4, __builtin_amdgcn_ds_read_tr16_b64_v4i16((__attribute__((address_space(3))) v4i16_t*)p));
}
__device__ __forceinline__ u32x4 pack8(const f32x16& a, int s) {
    u32x4 r;
    r[0] = cvtpk(a[8 * s + 0], a[8 * s + 1]); r[1] = cvtpk(a[8 * s + 2], a[8 * s + 3]);
    r[2] = cvtpk(a[8 * s + 4], a[8 * s + 5]); r[3] = cvtpk(a[8 * s + 6], a[8 * s + 7]);
    return r;
}
__device__ __forceinline__ u32x4 pack8n(const f32x16& a, int s) {
    u32x4 p = pack8(a, s);
    auto x = __builtin_amdgcn_permlane32_swap(p[0], p[2], false, false);
    auto y = __builtin_amdgcn_permlane32_swap(p[1], p[3], false, false);
    u32x4 r; r[0] = x[0]; r[1] = y[0]; r[2] = x[1]; r[3] = y[1];
    return r;
}
#define ST_AUX 1
__device__ __forceinline__ void st16(char* p, u32x4 v) {
    asm volatile("global_store_dwordx4 %0, %1, off sc0 sc1\n\ts_nop 1" ::"v"(p), "v"(v) : "memory");
}
#define WAITBAR(N) do { SBAR(); asm volatile("s_waitcnt vmcnt(" #N ")\n\ts_barrier" ::: "memory"); SBAR(); } while (0)
#define BAR() do { SBAR(); asm volatile("s_barrier" ::: "memory"); SBAR(); } while (0)

constexpr int K1_SLOT = 40960, K1_LDS = 4 * K1_SLOT;

#define K1_LOADER(DMA) do { \
        DMA(0); DMA(1); WAITBAR(20); DMA(2); DMA(3); WAITBAR(40); \
        WAITBAR(20); DMA(4); WAITBAR(20); DMA(5); WAITBAR(20); DMA(6); WAITBAR(20); WAITBAR(0); } while (0)

__global__ __launch_bounds__(640) void k1_proj(const u16* __restrict__ xA, const u16* __restrict__ wqkv,
                                               const u16* __restrict__ wo, const u16* __restrict__ xB,
                                               const u16* __restrict__ dre, const u16* __restrict__ dim,
                                               char* __restrict__ ws) {
    extern __shared__ __attribute__((aligned(16))) char lds[];
    const int tid = threadIdx.x, lane = tid & 63, l31 = lane & 31, hh = lane >> 5;
    const int wid = __builtin_amdgcn_readfirstlane(tid >> 6);
    const int b = blockIdx.x, xcd = b & 7, idx = b >> 3;
    const unsigned lds0 = (unsigned)(uintptr_t)lds;
    const unsigned xoff0 = (unsigned)(lane >> 3) * (DM * 2) + (unsigned)(((lane & 7) ^ (lane >> 4)) * 16);
    const unsigned xoff1 = (unsigned)(lane >> 3) * (DM * 2) + (unsigned)(((lane & 7) ^ (4 + (lane >> 4))) * 16) + 8 * DM * 2;
    const int sw = (l31 >> 1) & 7;
    const int ld = wid - 8;
    STAMP(0, 0);
    if (idx < 16) {
        const int hp = xcd, mb = idx;
        if (wid >= 8) {
            __builtin_amdgcn_s_setprio(3);
            const char* xs = (const char*)(xA + (size_t)(mb * 128 + ld * 64) * DM);
            const char* wsrc = (const char*)(wqkv + (size_t)(ld * 32) * 1344 + hp * 56);
            int wc = (lane & 7) ^ (((lane >> 4) & 1) * 4); wc = wc > 6 ? 6 : wc;
            const unsigned woff = (unsigned)(lane >> 3) * 2688 + (unsigned)wc * 16;
#define K1A_DMA(c) do { const unsigned sl = lds0 + ((c) & 3) * K1_SLOT; \
                _Pragma("unroll") for (int i = 0; i < 4; ++i) { \
                    glds(xoff0, xs + i * 16 * DM * 2 + (c) * 128, sl + ld * 8192 + i * 2048); \
                    glds(xoff1, xs + i * 16 * DM * 2 + (c) * 128, sl + ld * 8192 + i * 2048 + 1024); \
                    _Pragma("unroll") for (int g = 0; g < 3; ++g) \
                        glds(woff, wsrc + (size_t)((c) * 64 + i * 8) * 2688 + g * 896, sl + 16384 + g * 8192 + ld * 4096 + i * 1024); } } while (0)
            K1_LOADER(K1A_DMA);
            return;
        }
        const int tg = wid & 3, hs = wid >> 2, h = hp * 2 + hs;
        f32x16 acc[3];
#pragma unroll
        for (int g = 0; g < 3; ++g) acc[g] = f32x16{};
        const int wrow = 8 * hh + ((lane & 15) >> 2), wrot = ((lane >> 3) & 1) * 64, wcol = ((lane >> 4) & 1) * 32 + (lane & 3) * 8;
        const lds_cptr wqk = (lds_cptr)lds + 16384 + wrow * 128 + ((hs * 56 + wcol + wrot) & 127);
        const lds_cptr wv = (lds_cptr)lds + 16384 + 16384 + wrow * 128 + ((hs * 56 + 120 + wcol + wrot) & 127);
        const lds_cptr xr = (lds_cptr)lds + (tg * 32 + l31) * 128;
        bf16x8 xf[2], wf[2][3];
#define K1A_RD(ks) do { const int so = (((ks) >> 2) & 3) * K1_SLOT, kk = (ks) & 3; \
            xf[(ks) & 1] = ldsr128(xr + so + (((kk * 2 + hh) ^ sw) * 16)); \
            _Pragma("unroll") for (int g = 0; g < 3; ++g) { \
                lds_cptr p = (g < 2 ? wqk + g * 8192 : wv) + so + kk * 2048; \
                s16x4 lo = vtr(p), hi = vtr(p + 512); \
                wf[(ks) & 1][g] = (bf16x8){lo[0], lo[1], lo[2], lo[3], hi[0], hi[1], hi[2], hi[3]}; } } while (0)
        BAR();
        STAMP(0, 2);
        K1A_RD(0);
#pragma unroll
        for (int ks = 0; ks < 28; ++ks) {
            if ((ks & 3) == 3 && ks < 27) BAR();
            if (ks < 27) K1A_RD(ks + 1);
            SBAR();
            acc[0] = MFMA(wf[ks & 1][0], xf[ks & 1], acc[0]);
            acc[1] = MFMA(wf[ks & 1][1], xf[ks & 1], acc[1]);
            acc[2] = MFMA(xf[ks & 1], wf[ks & 1][2], acc[2]);
            SBAR();
        }
        STAMP(0, 4);
        const float qs = 0.18898223650461362f * 1.4426950408889634f;
        const int tt = mb * 4 + tg;
#pragma unroll
        for (int r = 0; r < 16; ++r) acc[0][r] *= qs;
        if (hh) {
#pragma unroll
            for (int r = 12; r < 16; ++r) { acc[0][r] = 0.f; acc[1][r] = (r < 15) ? 1.f : 0.f; }
        }
        if (l31 < 4) {
#pragma unroll
            for (int r = 0; r < 16; ++r) acc[2][r] = (l31 == 0) ? 1.f : 0.f;
        }
        char* qo = ws + WS_QF + (size_t)(h * 64 + tt) * 2048 + lane * 16;
        char* kvo = ws + WS_KVF + (size_t)(h * 64 + tt) * 4096 + lane * 16;
        st16(qo, pack8(acc[0], 0)); st16(qo + 1024, pack8(acc[0], 1));
        st16(kvo, pack8(acc[1], 0)); st16(kvo + 1024, pack8(acc[1], 1));
        st16(kvo + 2048, pack8(acc[2], 0)); st16(kvo + 3072, pack8(acc[2], 1));
        STAMP(0, 5);
    } else if (idx < 30) {
        const int j = idx - 16, im = xcd >> 2;
        const int sb = (xcd & 3) * 2 + (j & 1), et = j >> 1;
        if (wid >= 8) {
            __builtin_amdgcn_s_setprio(3);
            const char* xs = (const char*)(xB + (size_t)(sb * 256 + ld * 128) * DM);
            const char* dsrc = (const char*)((im ? dim : dre) + (size_t)(et * 64 + ld * 32) * DM);
#define K1B_DMA(c) do { const unsigned sl = lds0 + ((c) & 3) * K1_SLOT; \
                _Pragma("unroll") for (int i = 0; i < 2; ++i) { \
                    glds(xoff0, dsrc + i * 16 * DM * 2 + (c) * 128, sl + 32768 + ld * 4096 + i * 2048); \
                    glds(xoff1, dsrc + i * 16 * DM * 2 + (c) * 128, sl + 32768 + ld * 4096 + i * 2048 + 1024); } \
                _Pragma("unroll") for (int i = 0; i < 8; ++i) { \
                    glds(xoff0, xs + i * 16 * DM * 2 + (c) * 128, sl + ld * 16384 + i * 2048); \
                    glds(xoff1, xs + i * 16 * DM * 2 + (c) * 128, sl + ld * 16384 + i * 2048 + 1024); } } while (0)
            K1_LOADER(K1B_DMA);
            return;
        }
        f32x16 acc[2];
        acc[0] = f32x16{}; acc[1] = f32x16{};
        const lds_cptr xr = (lds_cptr)lds + (wid * 32 + l31) * 128;
        const lds_cptr dr = (lds_cptr)lds + 32768 + l31 * 128;
        bf16x8 xf[2], df[2][2];
#define K1B_RD(ks) do { const int so = (((ks) >> 2) & 3) * K1_SLOT, co = ((((ks) & 3) * 2 + hh) ^ sw) * 16; \
            xf[(ks) & 1] = ldsr128(xr + so + co); df[(ks) & 1][0] = ldsr128(dr + so + co); df[(ks) & 1][1] = ldsr128(dr + so + 4096 + co); } while (0)
        BAR();
        STAMP(0, 2);
        K1B_RD(0);
#pragma unroll
        for (int ks = 0; ks < 28; ++ks) {
            if ((ks & 3) == 3 && ks < 27) BAR();
            if (ks < 27) K1B_RD(ks + 1);
            SBAR();
            acc[0] = MFMA(xf[ks & 1], df[ks & 1][0], acc[0]);
            acc[1] = MFMA(xf[ks & 1], df[ks & 1][1], acc[1]);
            SBAR();
        }
        STAMP(0, 4);
        const float sg = im ? -1.f : 1.f;
        const int ks0 = im * 128 + sb * 16 + wid * 2;
#pragma unroll
        for (int nt = 0; nt < 2; ++nt) {
            const int ng = et * 2 + nt, nhalf = ng / 7, n7 = ng % 7;
#pragma unroll
            for (int r = 0; r < 16; ++r) acc[nt][r] *= sg;
#pragma unroll
            for (int s = 0; s < 2; ++s)
                st16(ws + WS_ABF + ((size_t)(nhalf * 256 + ks0 + s) * 7 + n7) * 1024 + lane * 16, pack8n(acc[nt], s));
        }
    } else if (wid < 8) {
        const int w0 = (xcd * 2 + (idx - 30)) * 8 + wid;
        u16 v[4][8];
#pragma unroll
        for (int i = 0; i < 4; ++i) {
            const int f = min(w0 + i * 128, 447);
            const int nt = f >> 5, ks = f & 31, h = ks >> 1, s = ks & 1;
#pragma unroll
            for (int j = 0; j < 8; ++j) {
                const int rho = 16 * s + 8 * (j >> 2) + 4 * hh + (j & 3);
                const u16 t = wo[(size_t)(h * 28 + max(rho - 4, 0)) * DM + nt * 32 + l31];
                v[i][j] = (rho >= 4) ? t : (u16)0;
            }
        }
#pragma unroll
        for (int i = 0; i < 4; ++i) {
            const int f = min(w0 + i * 128, 447);
            u32x4 o;
#pragma unroll
            for (int j = 0; j < 4; ++j) o[j] = (unsigned)v[i][2 * j] | ((unsigned)v[i][2 * j + 1] << 16);
            *(u32x4*)(ws + WS_WOF + (size_t)f * 1024 + lane * 16) = o;
        }
    }
}

constexpr int K2_KV = 0, K2_B = 65536, K2_A = 122880, K2_LDS = 155648;
constexpr float ATT_T = 30.f;
constexpr float ATT_LIM = 1.1529215e18f;

__device__ __forceinline__ float bf_trunc(float x) { return __uint_as_float(__float_as_uint(x) & 0xffff0000u); }
__device__ __forceinline__ bf16x8 set_ref(bf16x8 qf1, float mref, int hh) {
    const float nm = -mref;
    const float m0 = bf_trunc(nm), r1 = nm - m0, m1 = bf_trunc(r1), m2 = bf_trunc(r1 - m1);
    u32x4 q1 = __builtin_bit_cast(u32x4, qf1);
    if (hh) {
        q1[2] = (__float_as_uint(m0) >> 16) | (__float_as_uint(m1) & 0xffff0000u);
        q1[3] = (__float_as_uint(m2) >> 16);
    }
    return __builtin_bit_cast(bf16x8, q1);
}
__device__ __forceinline__ float rowmax(const f32x16& s) {
    float mx = s[0];
#pragma unroll
    for (int r = 1; r < 16; ++r) mx = fmaxf(mx, s[r]);
    auto rr = __builtin_amdgcn_permlane32_swap(__float_as_uint(mx), __float_as_uint(mx), false, false);
    return fmaxf(__uint_as_float(rr[0]), __uint_as_float(rr[1]));
}
#define EXP4(S, i) do { S[i] = __builtin_amdgcn_exp2f(S[i]); S[i + 1] = __builtin_amdgcn_exp2f(S[i + 1]); \
                        S[i + 2] = __builtin_amdgcn_exp2f(S[i + 2]); S[i + 3] = __builtin_amdgcn_exp2f(S[i + 3]); } while (0)

__global__ __launch_bounds__(640) void k2_attn_dft(const u16* __restrict__ cre, const u16* __restrict__ cim,
                                                   char* __restrict__ ws) {
    extern __shared__ __attribute__((aligned(16))) char lds[];
    const int tid = threadIdx.x, lane = tid & 63, l31 = lane & 31, hh = lane >> 5;
    const int wid = __builtin_amdgcn_readfirstlane(tid >> 6);
    const int b = blockIdx.x, xcd = b & 7, idx = b >> 3;
    const unsigned lds0 = (unsigned)(uintptr_t)lds;
    const int h = xcd * 2 + (idx & 1);
    const int split = xcd, mt = idx >> 1, nhalf = idx & 1;
    STAMP(1, 0);
    if (wid >= 8) {
        __builtin_amdgcn_s_setprio(3);
        const char* bsrc = ws + WS_ABF + (size_t)(nhalf * 256 + split * 32) * 7168;
        const unsigned lo = lane * 16;
        if (wid == 8) {
            const char* kvsrc = ws + WS_KVF + (size_t)h * 262144;
#define K2_L0(s) do { const unsigned sl = (s) & 3; \
                _Pragma("unroll") for (int i = 0; i < 4; ++i) glds4(lo, kvsrc + (size_t)(s) * 16384 + i * 4096, lds0 + K2_KV + sl * 16384 + i * 4096); \
                glds3(lo, bsrc + (size_t)(s) * 14336, lds0 + K2_B + sl * 14336); } while (0)
            K2_L0(0); K2_L0(1); WAITBAR(19); K2_L0(2); K2_L0(3);
            WAITBAR(38);
            for (int s = 4; s < 16; ++s) { WAITBAR(19);
 K2_L0(s);
 }
        } else {
            const char* cm = (const char*)((split < 4 ? cre : cim) + (size_t)(mt * 128) * SEQ + (split & 3) * 512);
            const unsigned ao = (unsigned)(lane >> 2) * 4096 + (unsigned)(((lane & 3) ^ ((lane >> 4) & 3)) * 16);
#define K2_L1(s) do { const unsigned sl = (s) & 3; \
                _Pragma("unroll") for (int i = 0; i < 8; ++i) glds(ao, cm + (size_t)i * 65536 + (s) * 64, lds0 + K2_A + sl * 8192 + i * 1024); \
                glds4(lo, bsrc + (size_t)(s) * 14336 + 3072, lds0 + K2_B + sl * 14336 + 3072); \
                glds4(lo, bsrc + (size_t)(s) * 14336 + 7168, lds0 + K2_B + sl * 14336 + 7168); \
                glds3(lo, bsrc + (size_t)(s) * 14336 + 11264, lds0 + K2_B + sl * 14336 + 11264); } while (0)
            K2_L1(0); K2_L1(1); WAITBAR(19); K2_L1(2); K2_L1(3);
            WAITBAR(38);
            for (int s = 4; s < 16; ++s) { WAITBAR(19);
 K2_L1(s);
 }
        }
        WAITBAR(19);
        WAITBAR(0);
        BAR();
    } else if (wid < 4) {
        const int qh = wid & 1, kh = wid >> 1;
        const int qt = (idx >> 1) * 4 + qh * 2;
        const char* qsrc = ws + WS_QF + (size_t)(h * 64 + qt) * 2048 + lane * 16;
        const bf16x8 qa0 = *(const bf16x8*)qsrc, qb0 = *(const bf16x8*)(qsrc + 2048);
        bf16x8 qa1 = *(const bf16x8*)(qsrc + 1024), qb1 = *(const bf16x8*)(qsrc + 3072);
        const f32x16 zero16 = f32x16{};
        const lds_cptr kv0 = (lds_cptr)lds + K2_KV + lane * 16;
        const lds_cptr kv = kv0 + kh * 4096;
        BAR();
        STAMP(1, 1);
        bf16x8 kA0 = ldsr128(kv0), kA1 = ldsr128(kv0 + 1024);
        f32x16 s = MFMA(kA0, qa0, zero16), sn = MFMA(kA0, qb0, zero16);
        s = MFMA(kA1, qa1, s); sn = MFMA(kA1, qb1, sn);
        kA0 = ldsr128(kv); kA1 = ldsr128(kv + 1024);
        bf16x8 vA0 = ldsr128(kv + 2048), vA1 = ldsr128(kv + 3072);
        bf16x8 kB0 = ldsr128(kv + 8192), kB1 = ldsr128(kv + 8192 + 1024), vB0, vB1;
        qa1 = set_ref(qa1, rowmax(s) + ATT_T, hh);
        qb1 = set_ref(qb1, rowmax(sn) + ATT_T, hh);
        s = MFMA(kA0, qa0, zero16);
        s = MFMA(kA1, qa1, s);
        f32x16 oa = f32x16{}, ob = f32x16{};
#define E1(S, i) S[i] = __builtin_amdgcn_exp2f(S[i])
#define UNIT(S, O, V0, V1, SN, K0, K1, Q0, Q1, L0, L1, LP) do { u32x4 p0, p1; \
            SBAR(); E1(S, 0); E1(S, 1); E1(S, 2); E1(S, 3); \
            SBAR(); SN = MFMA(K0, Q0, zero16); \
            SBAR(); E1(S, 4); E1(S, 5); p0[0] = cvtpk(S[0], S[1]); p0[1] = cvtpk(S[2], S[3]); E1(S, 6); E1(S, 7); \
            SBAR(); p0[2] = cvtpk(S[4], S[5]); p0[3] = cvtpk(S[6], S[7]); \
            SBAR(); E1(S, 8); L0 = ldsr128(LP); \
            SBAR(); O = MFMA(V0, __builtin_bit_cast(bf16x8, p0), O); \
            SBAR(); E1(S, 9); E1(S, 10); E1(S, 11); \
            SBAR(); SN = MFMA(K1, Q1, SN); \
            SBAR(); E1(S, 12); E1(S, 13); p1[0] = cvtpk(S[8], S[9]); p1[1] = cvtpk(S[10], S[11]); E1(S, 14); E1(S, 15); \
            SBAR(); p1[2] = cvtpk(S[12], S[13]); p1[3] = cvtpk(S[14], S[15]); \
            SBAR(); L1 = ldsr128(LP + 1024); \
            SBAR(); O = MFMA(V1, __builtin_bit_cast(bf16x8, p1), O); SBAR(); } while (0)
        for (int st = 0; st < 16; ++st) {
            const lds_cptr cur = kv + (st & 3) * 16384, nxt = kv + ((st + 1) & 3) * 16384;
            UNIT(s, oa, vA0, vA1, sn, kA0, kA1, qb0, qb1, vB0, vB1, cur + 8192 + 2048);
            if (st < 15) BAR();
            UNIT(sn, ob, vA0, vA1, s, kB0, kB1, qa0, qa1, kA0, kA1, nxt);
            UNIT(s, oa, vB0, vB1, sn, kB0, kB1, qb0, qb1, vA0, vA1, nxt + 2048);
            UNIT(sn, ob, vB0, vB1, s, kA0, kA1, qa0, qa1, kB0, kB1, nxt + 8192);
            if (st == 7) STAMP(1, 2);
        }
        STAMP(1, 3);
        f32x16 o;
        {
            char* xb = lds + K2_KV + 16384 + lane * 16;
#pragma unroll
            for (int g = 0; g < 4; ++g) {
                f32x4 t;
#pragma unroll
                for (int j = 0; j < 4; ++j) t[j] = kh ? oa[4 * g + j] : ob[4 * g + j];
                *(f32x4*)(xb + (wid ^ 2) * 4096 + g * 1024) = t;
            }
            asm volatile("s_waitcnt lgkmcnt(0)\n\ts_barrier" ::: "memory");
#pragma unroll
            for (int g = 0; g < 4; ++g) {
                const f32x4 t = *(const f32x4*)(xb + wid * 4096 + g * 1024);
#pragma unroll
                for (int j = 0; j < 4; ++j) o[4 * g + j] = (kh ? ob[4 * g + j] : oa[4 * g + j]) + t[j];
            }
        }
        {
            auto rr = __builtin_amdgcn_permlane32_swap(__float_as_uint(o[0]), __float_as_uint(o[0]), false, false);
            const float l = __uint_as_float(rr[0]);
            if (__builtin_expect(__any(!(l < ATT_LIM)), 0)) {
                const char* kvg = ws + WS_KVF + (size_t)h * 262144 + lane * 16;
                const bf16x8 qf0 = kh ? qb0 : qa0;
                bf16x8 qf1 = set_ref(kh ? qb1 : qa1, 0.f, hh);
                float mref = 0.f;
                o = zero16;
                for (int t = 0; t < 64; ++t) {
                    kA0 = *(const bf16x8*)(kvg + t * 4096); kA1 = *(const bf16x8*)(kvg + t * 4096 + 1024);
                    vA0 = *(const bf16x8*)(kvg + t * 4096 + 2048); vA1 = *(const bf16x8*)(kvg + t * 4096 + 3072);
                    s = MFMA(kA0, qf0, zero16);
                    s = MFMA(kA1, qf1, s);
                    float dl = rowmax(s);
                    if (t) dl = fmaxf(dl, 0.f);
                    const float mnew = mref + dl;
                    const float d2 = mnew - mref;
                    const float alpha = t ? __builtin_amdgcn_exp2f(-d2) : 1.f;
                    mref = mnew;
                    qf1 = set_ref(qf1, mref, hh);
#pragma unroll
                    for (int r = 0; r < 16; ++r) { s[r] = __builtin_amdgcn_exp2f(s[r] - d2); o[r] *= alpha; }
                    u32x4 p0 = pack8(s, 0), p1 = pack8(s, 1);
                    o = MFMA(vA0, __builtin_bit_cast(bf16x8, p0), o);
                    o = MFMA(vA1, __builtin_bit_cast(bf16x8, p1), o);
                }
            }
        }
        auto rr = __builtin_amdgcn_permlane32_swap(__float_as_uint(o[0]), __float_as_uint(o[0]), false, false);
        const float linv = 1.f / __uint_as_float(rr[0]);
#pragma unroll
        for (int r = 0; r < 16; ++r) o[r] *= linv;
        char* oo = ws + WS_OF + (size_t)((qt + kh) * 32 + h * 2) * 1024 + lane * 16;
        st16(oo, pack8(o, 0));
        st16(oo + 1024, pack8(o, 1));
        STAMP(1, 4);
    } else {
        const int w = wid - 4;
        f32x16 acc[7];
#pragma unroll
        for (int n = 0; n < 7; ++n) acc[n] = f32x16{};
        const int asw = (l31 >> 2) & 3;
        const lds_cptr a0 = (lds_cptr)lds + K2_A + (w * 32 + l31) * 64 + ((hh ^ asw) * 16);
        const lds_cptr a1 = (lds_cptr)lds + K2_A + (w * 32 + l31) * 64 + (((2 + hh) ^ asw) * 16);
        const lds_cptr bb = (lds_cptr)lds + K2_B + lane * 16;
        BAR();
        STAMP(1, 1);
        bf16x8 bq[7], af, an;
        af = ldsr128(a0);
#pragma unroll
        for (int n = 0; n < 7; ++n) bq[n] = ldsr128(bb + n * 1024);
        for (int st = 0; st < 16; ++st) {
            const int sl = st & 3, sn = (st + 1) & 3;
            const lds_cptr bc = bb + sl * 14336 + 7168, bn = bb + sn * 14336;
            an = ldsr128(a1 + sl * 8192);
#pragma unroll
            for (int n = 0; n < GEMM_N; ++n) {
                SBAR();
                acc[n] = MFMA(bq[n], af, acc[n]);
                bq[n] = ldsr128(bc + n * 1024);
            }
            if (st < 15) BAR();
            af = ldsr128(a0 + sn * 8192);
#pragma unroll
            for (int n = 0; n < GEMM_N; ++n) {
                SBAR();
                acc[n] = MFMA(bq[n], an, acc[n]);
                bq[n] = ldsr128(bn + n * 1024);
            }
        }
        STAMP(1, 3);
        char* yo = ws + WS_YP + ((size_t)(split * 64 + mt * 4 + w) * 14 + nhalf * 7) * 2048 + lane * 16;
#pragma unroll
        for (int n = 0; n < 7; ++n) {
            st16(yo + n * 2048, pack8n(acc[n], 0));
            st16(yo + n * 2048 + 1024, pack8n(acc[n], 1));
        }
        BAR();
        STAMP(1, 4);
    }
}

__device__ __forceinline__ float bflo(unsigned u) { return __uint_as_float(u << 16); }
__device__ __forceinline__ float bfhi(unsigned u) { return __uint_as_float(u & 0xffff0000u); }

__global__ __launch_bounds__(256) void k3_out(char* __restrict__ ws, u16* __restrict__ out) {
    __shared__ f32x4 red[4][3][4][64];
    const int tid = threadIdx.x, lane = tid & 63, l31 = lane & 31, hh = lane >> 5;
    const int wid = __builtin_amdgcn_readfirstlane(tid >> 6);
    const int b = blockIdx.x, xcd = b & 7, idx = b >> 3;
    STAMP(2, 0);
    if (idx < 28) {
        const int tb = xcd * 4 + (idx & 3), nb = idx >> 2;
        const char* ap = ws + WS_OF + (size_t)(tb * 2 * 32 + wid * 8) * 1024 + lane * 16;
        const char* bp = ws + WS_WOF + (size_t)(nb * 2 * 32 + wid * 8) * 1024 + lane * 16;
        bf16x8 af[2][8], bf[2][8];
#pragma unroll
        for (int k = 0; k < 8; ++k) {
            gload16(af[0][k], ap + k * 1024); gload16(bf[0][k], bp + k * 1024);
            gload16(af[1][k], ap + 32768 + k * 1024); gload16(bf[1][k], bp + 32768 + k * 1024);
        }
        f32x16 acc[4];
#pragma unroll
        for (int i = 0; i < 4; ++i) acc[i] = f32x16{};
#define K3_STEP(k, N) do { \
            asm volatile("s_waitcnt vmcnt(" #N ")" : "+v"(af[0][k]), "+v"(bf[0][k]), "+v"(af[1][k]), "+v"(bf[1][k]), \
                         "+v"(af[0][k + 1]), "+v"(bf[0][k + 1]), "+v"(af[1][k + 1]), "+v"(bf[1][k + 1]) :: "memory"); \
            _Pragma("unroll") for (int kk = k; kk < k + 2; ++kk) \
            _Pragma("unroll") for (int i = 0; i < 4; ++i) acc[i] = MFMA(bf[i & 1][kk], af[i >> 1][kk], acc[i]); } while (0)
        K3_STEP(0, 24); K3_STEP(2, 16); K3_STEP(4, 8); K3_STEP(6, 0);
        STAMP(2, 1);
#pragma unroll
        for (int i = 0; i < 4; ++i) {
            if (i != wid) {
                const int slot = (wid - i - 1) & 3;
#pragma unroll
                for (int g = 0; g < 4; ++g)
                    red[i][slot][g][lane] = (f32x4){acc[i][4 * g], acc[i][4 * g + 1], acc[i][4 * g + 2], acc[i][4 * g + 3]};
            }
        }
        __syncthreads();
        f32x16 f = (wid == 0) ? acc[0] : (wid == 1) ? acc[1] : (wid == 2) ? acc[2] : acc[3];
#pragma unroll
        for (int s = 0; s < 3; ++s)
#pragma unroll
            for (int g = 0; g < 4; ++g) {
                const f32x4 t = red[wid][s][g][lane];
#pragma unroll
                for (int j = 0; j < 4; ++j) f[4 * g + j] += t[j];
            }
        const int t = tb * 64 + (wid >> 1) * 32 + l31, n0 = nb * 64 + (wid & 1) * 32 + 8 * hh;
        *(u32x4*)(out + (size_t)t * 896 + n0) = pack8n(f, 0);
        *(u32x4*)(out + (size_t)t * 896 + n0 + 16) = pack8n(f, 1);
        STAMP(2, 2);
    } else {
        const int task = ((idx - 28) * 8 + xcd) * 4 + wid;
        const int tt = task / 14, ng = task % 14;
        const char* yp = ws + WS_YP + ((size_t)tt * 14 + ng) * 2048 + lane * 16;
        u32x4 v[8][2];
#pragma unroll
        for (int s = 0; s < 8; ++s) {
            v[s][0] = *(const u32x4*)(yp + (size_t)s * 64 * 14 * 2048);
            v[s][1] = *(const u32x4*)(yp + (size_t)s * 64 * 14 * 2048 + 1024);
        }
        const int t = tt * 32 + l31, n0 = 448 + ng * 32 + 8 * hh;
#pragma unroll
        for (int i = 0; i < 2; ++i) {
            u32x4 o;
#pragma unroll
            for (int j = 0; j < 4; ++j) {
                float lo = 0.f, hi = 0.f;
#pragma unroll
                for (int s = 0; s < 8; ++s) { lo += bflo(v[s][i][j]); hi += bfhi(v[s][i][j]); }
                o[j] = cvtpk(lo, hi);
            }
            *(u32x4*)(out + (size_t)t * 896 + n0 + 16 * i) = o;
        }
        STAMP(2, 2);
    }
}

extern "C" void kernel_launch(void* const* d_in, const int* in_sizes, int n_in, void* d_out, int out_size, void* d_ws,
                              size_t ws_size, hipStream_t stream) {
    const u16* xA = (const u16*)d_in[0];
    const u16* wqkv = (const u16*)d_in[1];
    const u16* wo = (const u16*)d_in[2];
    const u16* xB = (const u16*)d_in[3];
    const u16* cre = (const u16*)d_in[4];
    const u16* cim = (const u16*)d_in[5];
    const u16* dre = (const u16*)d_in[6];
    const u16* dim = (const u16*)d_in[7];
    char* ws = (char*)d_ws;
    (void)hipFuncSetAttribute((const void*)k1_proj, hipFuncAttributeMaxDynamicSharedMemorySize, K1_LDS);
    (void)hipFuncSetAttribute((const void*)k2_attn_dft, hipFuncAttributeMaxDynamicSharedMemorySize, K2_LDS);
    k1_proj<<<256, 640, K1_LDS, stream>>>(xA, wqkv, wo, xB, dre, dim, ws);
    k2_attn_dft<<<256, 640, K2_LDS, stream>>>(cre, cim, ws);
    k3_out<<<448, 256, 0, stream>>>(ws, (u16*)d_out);
}
```

```cpp
#include <hip/hip_runtime.h>
#include <stdint.h>

typedef __attribute__((ext_vector_type(8))) short bf16x8;
typedef __attribute__((ext_vector_type(4))) short s16x4;
typedef __attribute__((ext_vector_type(16))) float f32x16;
typedef __attribute__((ext_vector_type(4))) unsigned u32x4;
typedef __attribute__((ext_vector_type(2))) unsigned u32x2;
typedef __attribute__((ext_vector_type(2))) float f32x2;
typedef __attribute__((ext_vector_type(4))) float f32x4;
typedef __attribute__((ext_vector_type(2))) __bf16 bf16x2_t;
typedef unsigned short u16;
typedef __attribute__((address_space(3))) const char* lds_cptr;
typedef short v4i16_t __attribute__((ext_vector_type(4)));

#define MFMA(a, b, c) __builtin_amdgcn_mfma_f32_32x32x16_bf16(a, b, c, 0, 0, 0)
#define ATT_NT 4
#define GEMM_N 7
#define NO_BAR 0
#define SBAR() __builtin_amdgcn_sched_barrier(0)

constexpr int SEQ = 2048, DM = 448;
#define STAMP(k, i)
constexpr size_t WS_QF = 0;
constexpr size_t WS_KVF = WS_QF + 2097152;
constexpr size_t WS_ABF = WS_KVF + 4194304;
constexpr size_t WS_WOF = WS_ABF + 3670016;
constexpr size_t WS_OF = WS_WOF + 458752;
constexpr size_t WS_YP = WS_OF + 2097152;

__device__ __forceinline__ unsigned cvtpk(float lo, float hi) {
    f32x2 v = {lo, hi};
    return __builtin_bit_cast(unsigned, __builtin_convertvector(v, bf16x2_t));
}
__device__ __forceinline__ void glds(unsigned voff, const void* sbase, unsigned lds_base) {
    asm volatile("s_mov_b32 m0, %2\n\ts_nop 0\n\tglobal_load_lds_dwordx4 %0, %1" ::"v"(voff), "s"(sbase), "s"(lds_base) : "memory");
}
__device__ __forceinline__ void glds4(unsigned voff, const void* sbase, unsigned lds_base) {
    asm volatile("s_mov_b32 m0, %2\n\ts_nop 0\n\tglobal_load_lds_dwordx4 %0, %1\n\tglobal_load_lds_dwordx4 %0, %1 offset:1024\n\t"
                 "global_load_lds_dwordx4 %0, %1 offset:2048\n\tglobal_load_lds_dwordx4 %0, %1 offset:3072"
                 ::"v"(voff), "s"(sbase), "s"(lds_base) : "memory");
}
__device__ __forceinline__ void glds3(unsigned voff, const void* sbase, unsigned lds_base) {
    asm volatile("s_mov_b32 m0, %2\n\ts_nop 0\n\tglobal_load_lds_dwordx4 %0, %1\n\tglobal_load_lds_dwordx4 %0, %1 offset:1024\n\t"
                 "global_load_lds_dwordx4 %0, %1 offset:2048"
                 ::"v"(voff), "s"(sbase), "s"(lds_base) : "memory");
}
__device__ __forceinline__ void gload16(bf16x8& d, const void* g) {
    asm volatile("global_load_dwordx4 %0, %1, off" : "=v"(d) : "v"(g) : "memory");
}
__device__ __forceinline__ bf16x8 ldsr128(lds_cptr p) { return *(const __attribute__((address_space(3))) bf16x8*)p; }
__device__ __forceinline__ s16x4 vtr(lds_cptr p) {
    return __builtin_bit_cast(s16x4, __builtin_amdgcn_ds_read_tr16_b64_v4i16((__attribute__((address_space(3))) v4i16_t*)p));
}
__device__ __forceinline__ u32x4 pack8(const f32x16& a, int s) {
    u32x4 r;
    r[0] = cvtpk(a[8 * s + 0], a[8 * s + 1]); r[1] = cvtpk(a[8 * s + 2], a[8 * s + 3]);
    r[2] = cvtpk(a[8 * s + 4], a[8 * s + 5]); r[3] = cvtpk(a[8 * s + 6], a[8 * s + 7]);
    return r;
}
__device__ __forceinline__ u32x4 pack8n(const f32x16& a, int s) {
    u32x4 p = pack8(a, s);
    auto x = __builtin_amdgcn_permlane32_swap(p[0], p[2], false, false);
    auto y = __builtin_amdgcn_permlane32_swap(p[1], p[3], false, false);
    u32x4 r; r[0] = x[0]; r[1] = y[0]; r[2] = x[1]; r[3] = y[1];
    return r;
}
#define ST_AUX 1
__device__ __forceinline__ void st16(char* p, u32x4 v) {
    asm volatile("global_store_dwordx4 %0, %1, off sc0 sc1\n\ts_nop 1" ::"v"(p), "v"(v) : "memory");
}
#define WAITBAR(N) do { SBAR(); asm volatile("s_waitcnt vmcnt(" #N ")\n\ts_barrier" ::: "memory"); SBAR(); } while (0)
#define BAR() do { SBAR(); asm volatile("s_barrier" ::: "memory"); SBAR(); } while (0)

__device__ __forceinline__ float bflo(unsigned u) { return __uint_as_float(u << 16); }
__device__ __forceinline__ float bfhi(unsigned u) { return __uint_as_float(u & 0xffff0000u); }

__global__ __launch_bounds__(256) void k3_out(char* __restrict__ ws, u16* __restrict__ out) {
    __shared__ f32x4 red[4][3][4][64];
    const int tid = threadIdx.x, lane = tid & 63, l31 = lane & 31, hh = lane >> 5;
    const int wid = __builtin_amdgcn_readfirstlane(tid >> 6);
    const int b = blockIdx.x, xcd = b & 7, idx = b >> 3;
    STAMP(2, 0);
    if (idx < 28) {
        const int tb = xcd * 4 + (idx & 3), nb = idx >> 2;
        const char* ap = ws + WS_OF + (size_t)(tb * 2 * 32 + wid * 8) * 1024 + lane * 16;
        const char* bp = ws + WS_WOF + (size_t)(nb * 2 * 32 + wid * 8) * 1024 + lane * 16;
        bf16x8 af[2][8], bf[2][8];
#pragma unroll
        for (int k = 0; k < 8; ++k) {
            gload16(af[0][k], ap + k * 1024); gload16(bf[0][k], bp + k * 1024);
            gload16(af[1][k], ap + 32768 + k * 1024); gload16(bf[1][k], bp + 32768 + k * 1024);
        }
        f32x16 acc[4];
#pragma unroll
        for (int i = 0; i < 4; ++i) acc[i] = f32x16{};
#define K3_STEP(k, N) do { \
            asm volatile("s_waitcnt vmcnt(" #N ")" : "+v"(af[0][k]), "+v"(bf[0][k]), "+v"(af[1][k]), "+v"(bf[1][k]), \
                         "+v"(af[0][k + 1]), "+v"(bf[0][k + 1]), "+v"(af[1][k + 1]), "+v"(bf[1][k + 1]) :: "memory"); \
            _Pragma("unroll") for (int kk = k; kk < k + 2; ++kk) \
            _Pragma("unroll") for (int i = 0; i < 4; ++i) acc[i] = MFMA(bf[i & 1][kk], af[i >> 1][kk], acc[i]); } while (0)
        K3_STEP(0, 24); K3_STEP(2, 16); K3_STEP(4, 8); K3_STEP(6, 0);
        STAMP(2, 1);
#pragma unroll
        for (int i = 0; i < 4; ++i) {
            if (i != wid) {
                const int slot = (wid - i - 1) & 3;
#pragma unroll
                for (int g = 0; g < 4; ++g)
                    red[i][slot][g][lane] = (f32x4){acc[i][4 * g], acc[i][4 * g + 1], acc[i][4 * g + 2], acc[i][4 * g + 3]};
            }
        }
        __syncthreads();
        f32x16 f = (wid == 0) ? acc[0] : (wid == 1) ? acc[1] : (wid == 2) ? acc[2] : acc[3];
#pragma unroll
        for (int s = 0; s < 3; ++s)
#pragma unroll
            for (int g = 0; g < 4; ++g) {
                const f32x4 t = red[wid][s][g][lane];
#pragma unroll
                for (int j = 0; j < 4; ++j) f[4 * g + j] += t[j];
            }
        const int t = tb * 64 + (wid >> 1) * 32 + l31, n0 = nb * 64 + (wid & 1) * 32 + 8 * hh;
        *(u32x4*)(out + (size_t)t * 896 + n0) = pack8n(f, 0);
        *(u32x4*)(out + (size_t)t * 896 + n0 + 16) = pack8n(f, 1);
        STAMP(2, 2);
    } else {
        const int task = ((idx - 28) * 8 + xcd) * 4 + wid;
        const int tt = task / 14, ng = task % 14;
        const char* yp = ws + WS_YP + ((size_t)tt * 14 + ng) * 2048 + lane * 16;
        u32x4 v[8][2];
#pragma unroll
        for (int s = 0; s < 8; ++s) {
            v[s][0] = *(const u32x4*)(yp + (size_t)s * 64 * 14 * 2048);
            v[s][1] = *(const u32x4*)(yp + (size_t)s * 64 * 14 * 2048 + 1024);
        }
        const int t = tt * 32 + l31, n0 = 448 + ng * 32 + 8 * hh;
#pragma unroll
        for (int i = 0; i < 2; ++i) {
            u32x4 o;
#pragma unroll
            for (int j = 0; j < 4; ++j) {
                float lo = 0.f, hi = 0.f;
#pragma unroll
                for (int s = 0; s < 8; ++s) { lo += bflo(v[s][i][j]); hi += bfhi(v[s][i][j]); }
                o[j] = cvtpk(lo, hi);
            }
            *(u32x4*)(out + (size_t)t * 896 + n0 + 16 * i) = o;
        }
        STAMP(2, 2);
    }
}

constexpr int K2_KV = 0, K2_B = 65536, K2_A = 122880, K2_LDS = 155648;
constexpr float ATT_T = 30.f;
constexpr float ATT_LIM = 1.1529215e18f;

__device__ __forceinline__ float bf_trunc(float x) { return __uint_as_float(__float_as_uint(x) & 0xffff0000u); }
__device__ __forceinline__ bf16x8 set_ref(bf16x8 qf1, float mref, int hh) {
    const float nm = -mref;
    const float m0 = bf_trunc(nm), r1 = nm - m0, m1 = bf_trunc(r1), m2 = bf_trunc(r1 - m1);
    u32x4 q1 = __builtin_bit_cast(u32x4, qf1);
    if (hh) {
        q1[2] = (__float_as_uint(m0) >> 16) | (__float_as_uint(m1) & 0xffff0000u);
        q1[3] = (__float_as_uint(m2) >> 16);
    }
    return __builtin_bit_cast(bf16x8, q1);
}
__device__ __forceinline__ float rowmax(const f32x16& s) {
    float mx = s[0];
#pragma unroll
    for (int r = 1; r < 16; ++r) mx = fmaxf(mx, s[r]);
    auto rr = __builtin_amdgcn_permlane32_swap(__float_as_uint(mx), __float_as_uint(mx), false, false);
    return fmaxf(__uint_as_float(rr[0]), __uint_as_float(rr[1]));
}
#define EXP4(S, i) do { S[i] = __builtin_amdgcn_exp2f(S[i]); S[i + 1] = __builtin_amdgcn_exp2f(S[i + 1]); \
                        S[i + 2] = __builtin_amdgcn_exp2f(S[i + 2]); S[i + 3] = __builtin_amdgcn_exp2f(S[i + 3]); } while (0)

__global__ __launch_bounds__(640) void k2_attn_dft(const u16* __restrict__ cre, const u16* __restrict__ cim,
                                                   char* __restrict__ ws) {
    extern __shared__ __attribute__((aligned(16))) char lds[];
    const int tid = threadIdx.x, lane = tid & 63, l31 = lane & 31, hh = lane >> 5;
    const int wid = __builtin_amdgcn_readfirstlane(tid >> 6);
    const int b = blockIdx.x, xcd = b & 7, idx = b >> 3;
    const unsigned lds0 = (unsigned)(uintptr_t)lds;
    const int h = xcd * 2 + (idx & 1);
    const int split = xcd, mt = idx >> 1, nhalf = idx & 1;
    STAMP(1, 0);
    if (wid >= 8) {
#define LD_PRIO 3
        __builtin_amdgcn_s_setprio(LD_PRIO);
        const char* bsrc = ws + WS_ABF + (size_t)(nhalf * 256 + split * 32) * 7168;
        const unsigned lo = lane * 16;
#define ROT2 0
#define SR(s) (ROT2 ? (((s) + mt) & 15) : (s))
        if (wid == 8) {
            const char* kvsrc = ws + WS_KVF + (size_t)h * 262144;
#define K2_L0(s) do { const unsigned sl = (s) & 3; \
                _Pragma("unroll") for (int i = 0; i < 4; ++i) glds4(lo, kvsrc + (size_t)(s) * 16384 + i * 4096, lds0 + K2_KV + sl * 16384 + i * 4096); \
                glds3(lo, bsrc + (size_t)SR(s) * 14336, lds0 + K2_B + sl * 14336); } while (0)
            K2_L0(0); K2_L0(1); WAITBAR(19); K2_L0(2);
            WAITBAR(19); K2_L0(3);
            for (int s = 4; s < 16; ++s) { if (!NO_BAR) WAITBAR(19);
 K2_L0(s);
 }
        } else {
            const char* cm = (const char*)((split < 4 ? cre : cim) + (size_t)(mt * 128) * SEQ + (split & 3) * 512);
            const unsigned ao = (unsigned)(lane >> 2) * 4096 + (unsigned)(((lane & 3) ^ ((lane >> 4) & 3)) * 16);
#define K2_L1(s) do { const unsigned sl = (s) & 3; \
                _Pragma("unroll") for (int i = 0; i < 8; ++i) glds(ao, cm + (size_t)i * 65536 + SR(s) * 64, lds0 + K2_A + sl * 8192 + i * 1024); \
                glds4(lo, bsrc + (size_t)SR(s) * 14336 + 3072, lds0 + K2_B + sl * 14336 + 3072); \
                glds4(lo, bsrc + (size_t)SR(s) * 14336 + 7168, lds0 + K2_B + sl * 14336 + 7168); \
                glds3(lo, bsrc + (size_t)SR(s) * 14336 + 11264, lds0 + K2_B + sl * 14336 + 11264); } while (0)
            K2_L1(0); K2_L1(1); WAITBAR(19); K2_L1(2);
            WAITBAR(19); K2_L1(3);
            for (int s = 4; s < 16; ++s) { if (!NO_BAR) WAITBAR(19);
 K2_L1(s);
 }
        }
        WAITBAR(19);
        WAITBAR(0);
        BAR();
    } else if (wid < 4) {
        const int qh = wid & 1, kh = wid >> 1;
        const int qt = (idx >> 1) * 4 + qh * 2;
        const char* qsrc = ws + WS_QF + (size_t)(h * 64 + qt) * 2048 + lane * 16;
        const bf16x8 qa0 = *(const bf16x8*)qsrc, qb0 = *(const bf16x8*)(qsrc + 2048);
        bf16x8 qa1 = *(const bf16x8*)(qsrc + 1024), qb1 = *(const bf16x8*)(qsrc + 3072);
        const f32x16 zero16 = f32x16{};
        const lds_cptr kv0 = (lds_cptr)lds + K2_KV + lane * 16;
        const lds_cptr kv = kv0 + kh * 4096;
        BAR();
        STAMP(1, 1);
        bf16x8 kA0 = ldsr128(kv0), kA1 = ldsr128(kv0 + 1024);
        f32x16 s = MFMA(kA0, qa0, zero16), sn = MFMA(kA0, qb0, zero16);
        s = MFMA(kA1, qa1, s); sn = MFMA(kA1, qb1, sn);
        kA0 = ldsr128(kv); kA1 = ldsr128(kv + 1024);
        bf16x8 vA0 = ldsr128(kv + 2048), vA1 = ldsr128(kv + 3072);
        bf16x8 kB0 = ldsr128(kv + 8192), kB1 = ldsr128(kv + 8192 + 1024), vB0, vB1;
        qa1 = set_ref(qa1, rowmax(s) + ATT_T, hh);
        qb1 = set_ref(qb1, rowmax(sn) + ATT_T, hh);
        s = MFMA(kA0, qa0, zero16);
        s = MFMA(kA1, qa1, s);
        f32x16 oa = f32x16{}, ob = f32x16{};
#define E1(S, i) S[i] = __builtin_amdgcn_exp2f(S[i])
#define UNIT(S, O, V0, V1, SN, K0, K1, Q0, Q1, L0, L1, LP) do { u32x4 p0, p1; \
            SBAR(); E1(S, 0); E1(S, 1); E1(S, 2); E1(S, 3); \
            SBAR(); SN = MFMA(K0, Q0, zero16); \
            SBAR(); E1(S, 4); E1(S, 5); p0[0] = cvtpk(S[0], S[1]); p0[1] = cvtpk(S[2], S[3]); E1(S, 6); E1(S, 7); \
            SBAR(); p0[2] = cvtpk(S[4], S[5]); p0[3] = cvtpk(S[6], S[7]); \
            SBAR(); E1(S, 8); L0 = ldsr128(LP); \
            SBAR(); O = MFMA(V0, __builtin_bit_cast(bf16x8, p0), O); \
            SBAR(); E1(S, 9); E1(S, 10); E1(S, 11); \
            SBAR(); SN = MFMA(K1, Q1, SN); \
            SBAR(); E1(S, 12); E1(S, 13); p1[0] = cvtpk(S[8], S[9]); p1[1] = cvtpk(S[10], S[11]); E1(S, 14); E1(S, 15); \
            SBAR(); p1[2] = cvtpk(S[12], S[13]); p1[3] = cvtpk(S[14], S[15]); \
            SBAR(); L1 = ldsr128(LP + 1024); \
            SBAR(); O = MFMA(V1, __builtin_bit_cast(bf16x8, p1), O); SBAR(); } while (0)
        for (int st = 0; st < 16; ++st) {
            const lds_cptr cur = kv + (st & 3) * 16384, nxt = kv + ((st + 1) & 3) * 16384;
            UNIT(s, oa, vA0, vA1, sn, kA0, kA1, qb0, qb1, vB0, vB1, cur + 8192 + 2048);
            if (st < 15 && (!NO_BAR || st == 0 || st > 12)) BAR();
            UNIT(sn, ob, vA0, vA1, s, kB0, kB1, qa0, qa1, kA0, kA1, nxt);
            UNIT(s, oa, vB0, vB1, sn, kB0, kB1, qb0, qb1, vA0, vA1, nxt + 2048);
            UNIT(sn, ob, vB0, vB1, s, kA0, kA1, qa0, qa1, kB0, kB1, nxt + 8192);
            if (st == 7) STAMP(1, 2);
        }
        STAMP(1, 3);
        f32x16 o;
        {
            char* xb = lds + K2_KV + 16384 + lane * 16;
#pragma unroll
            for (int g = 0; g < 4; ++g) {
                f32x4 t;
#pragma unroll
                for (int j = 0; j < 4; ++j) t[j] = kh ? oa[4 * g + j] : ob[4 * g + j];
                *(f32x4*)(xb + (wid ^ 2) * 4096 + g * 1024) = t;
            }
            asm volatile("s_waitcnt lgkmcnt(0)\n\ts_barrier" ::: "memory");
#pragma unroll
            for (int g = 0; g < 4; ++g) {
                const f32x4 t = *(const f32x4*)(xb + wid * 4096 + g * 1024);
#pragma unroll
                for (int j = 0; j < 4; ++j) o[4 * g + j] = (kh ? ob[4 * g + j] : oa[4 * g + j]) + t[j];
            }
        }
        {
            auto rr = __builtin_amdgcn_permlane32_swap(__float_as_uint(o[0]), __float_as_uint(o[0]), false, false);
            const float l = __uint_as_float(rr[0]);
            if (__builtin_expect(__any(!(l < ATT_LIM)), 0)) {
                const char* kvg = ws + WS_KVF + (size_t)h * 262144 + lane * 16;
                const bf16x8 qf0 = kh ? qb0 : qa0;
                bf16x8 qf1 = set_ref(kh ? qb1 : qa1, 0.f, hh);
                float mref = 0.f;
                o = zero16;
                for (int t = 0; t < 64; ++t) {
                    kA0 = *(const bf16x8*)(kvg + t * 4096); kA1 = *(const bf16x8*)(kvg + t * 4096 + 1024);
                    vA0 = *(const bf16x8*)(kvg + t * 4096 + 2048); vA1 = *(const bf16x8*)(kvg + t * 4096 + 3072);
                    s = MFMA(kA0, qf0, zero16);
                    s = MFMA(kA1, qf1, s);
                    float dl = rowmax(s);
                    if (t) dl = fmaxf(dl, 0.f);
                    const float mnew = mref + dl;
                    const float d2 = mnew - mref;
                    const float alpha = t ? __builtin_amdgcn_exp2f(-d2) : 1.f;
                    mref = mnew;
                    qf1 = set_ref(qf1, mref, hh);
#pragma unroll
                    for (int r = 0; r < 16; ++r) { s[r] = __builtin_amdgcn_exp2f(s[r] - d2); o[r] *= alpha; }
                    u32x4 p0 = pack8(s, 0), p1 = pack8(s, 1);
                    o = MFMA(vA0, __builtin_bit_cast(bf16x8, p0), o);
                    o = MFMA(vA1, __builtin_bit_cast(bf16x8, p1), o);
                }
            }
        }
        auto rr = __builtin_amdgcn_permlane32_swap(__float_as_uint(o[0]), __float_as_uint(o[0]), false, false);
        const float linv = 1.f / __uint_as_float(rr[0]);
#pragma unroll
        for (int r = 0; r < 16; ++r) o[r] *= linv;
        char* oo = ws + WS_OF + (size_t)((qt + kh) * 32 + h * 2) * 1024 + lane * 16;
        st16(oo, pack8(o, 0));
        st16(oo + 1024, pack8(o, 1));
        STAMP(1, 4);
    } else {
        const int w = wid - 4;
        f32x16 acc[7];
#pragma unroll
        for (int n = 0; n < 7; ++n) acc[n] = f32x16{};
        const int asw = (l31 >> 2) & 3;
        const lds_cptr a0 = (lds_cptr)lds + K2_A + (w * 32 + l31) * 64 + ((hh ^ asw) * 16);
        const lds_cptr a1 = (lds_cptr)lds + K2_A + (w * 32 + l31) * 64 + (((2 + hh) ^ asw) * 16);
        const lds_cptr bb = (lds_cptr)lds + K2_B + lane * 16;
        BAR();
        STAMP(1, 1);
        bf16x8 bq[7], af, an;
        af = ldsr128(a0);
#pragma unroll
        for (int n = 0; n < 7; ++n) bq[n] = ldsr128(bb + n * 1024);
        for (int st = 0; st < 16; ++st) {
            const int sl = st & 3, sn = (st + 1) & 3;
            const lds_cptr bc = bb + sl * 14336 + 7168, bn = bb + sn * 14336;
            an = ldsr128(a1 + sl * 8192);
#pragma unroll
            for (int n = 0; n < GEMM_N; ++n) {
                SBAR();
                acc[n] = MFMA(bq[n], af, acc[n]);
                bq[n] = ldsr128(bc + n * 1024);
            }
            if (st < 15 && (!NO_BAR || st == 0 || st > 12)) BAR();
            af = ldsr128(a0 + sn * 8192);
#pragma unroll
            for (int n = 0; n < GEMM_N; ++n) {
                SBAR();
                acc[n] = MFMA(bq[n], an, acc[n]);
                bq[n] = ldsr128(bn + n * 1024);
            }
        }
        STAMP(1, 3);
        char* yo = ws + WS_YP + ((size_t)(split * 64 + mt * 4 + w) * 14 + nhalf * 7) * 2048 + lane * 16;
#pragma unroll
        for (int n = 0; n < 7; ++n) {
            st16(yo + n * 2048, pack8n(acc[n], 0));
            st16(yo + n * 2048 + 1024, pack8n(acc[n], 1));
        }
        BAR();
        STAMP(1, 4);
    }
}

constexpr int K1_SLOT = 40960, K1_LDS = 4 * K1_SLOT;

#define K1_LOADER(DMA, N, N2) do { \
        DMA(0); DMA(1); WAITBAR(N); DMA(2); DMA(3); WAITBAR(N2); \
        WAITBAR(N); DMA(4); WAITBAR(N); DMA(5); WAITBAR(N); DMA(6); WAITBAR(N); WAITBAR(0); } while (0)
#define TRFRAG(d, p) do { s16x4 lo_ = vtr(p), hi_ = vtr((p) + 512); \
        d = (bf16x8){lo_[0], lo_[1], lo_[2], lo_[3], hi_[0], hi_[1], hi_[2], hi_[3]}; } while (0)

__global__ __launch_bounds__(640) void k1_proj(const u16* __restrict__ xA, const u16* __restrict__ wqkv,
                                               const u16* __restrict__ wo, const u16* __restrict__ xB,
                                               const u16* __restrict__ dre, const u16* __restrict__ dim,
                                               char* __restrict__ ws) {
    extern __shared__ __attribute__((aligned(16))) char lds[];
    const int tid = threadIdx.x, lane = tid & 63, l31 = lane & 31, hh = lane >> 5;
    const int wid = __builtin_amdgcn_readfirstlane(tid >> 6);
    const int b = blockIdx.x, xcd = b & 7, idx = b >> 3;
    const unsigned lds0 = (unsigned)(uintptr_t)lds;
    const unsigned xoff0 = (unsigned)(lane >> 3) * (DM * 2) + (unsigned)(((lane & 7) ^ (lane >> 4)) * 16);
    const unsigned xoff1 = (unsigned)(lane >> 3) * (DM * 2) + (unsigned)(((lane & 7) ^ (4 + (lane >> 4))) * 16) + 8 * DM * 2;
    const int sw = (l31 >> 1) & 7;
    const int ld = wid - 8;
    const int hp = (xcd & 1) * 4 + (idx & 3);
    int wc = (lane & 7) ^ (((lane >> 4) & 1) * 4); wc = wc > 6 ? 6 : wc;
    const unsigned woff = (unsigned)(lane >> 3) * 2688 + (unsigned)wc * 16;
    const int wrow = 8 * hh + ((lane & 15) >> 2), wrot = ((lane >> 3) & 1) * 64, wcol = ((lane >> 4) & 1) * 32 + (lane & 3) * 8;
#define ROT1 3
    const int rot = ROT1 ? (idx * ROT1 + xcd) % 7 : 0;
#define CC(c) ((c) + rot >= 7 ? (c) + rot - 7 : (c) + rot)
    STAMP(0, 0);
    if (idx < 16) {
        const int mb = (xcd >> 1) * 4 + (idx >> 2);
        if (wid >= 8) {
            __builtin_amdgcn_s_setprio(3);
            const char* xs = (const char*)(xA + (size_t)(mb * 128 + ld * 64) * DM);
            const char* wsrc = (const char*)(wqkv + (size_t)(ld * 32) * 1344 + hp * 56);
#define K1A_DMA(c) do { const unsigned sl = lds0 + ((c) & 3) * K1_SLOT; \
                _Pragma("unroll") for (int i = 0; i < 4; ++i) { \
                    glds(xoff0, xs + i * 16 * DM * 2 + CC(c) * 128, sl + ld * 8192 + i * 2048); \
                    glds(xoff1, xs + i * 16 * DM * 2 + CC(c) * 128, sl + ld * 8192 + i * 2048 + 1024); \
                    _Pragma("unroll") for (int g = 0; g < 2; ++g) \
                        glds(woff, wsrc + (size_t)(CC(c) * 64 + i * 8) * 2688 + g * 896, sl + 16384 + g * 8192 + ld * 4096 + i * 1024); } } while (0)
            K1_LOADER(K1A_DMA, 16, 32);
            return;
        }
        const int tg = wid & 3, hs = wid >> 2, h = hp * 2 + hs;
        f32x16 acc[2];
        acc[0] = f32x16{}; acc[1] = f32x16{};
        const lds_cptr wqk = (lds_cptr)lds + 16384 + wrow * 128 + ((hs * 56 + wcol + wrot) & 127);
        const lds_cptr xr = (lds_cptr)lds + (tg * 32 + l31) * 128;
        bf16x8 xf[2], wf[2][2];
#define K1A_RD(ks) do { const int so = (((ks) >> 2) & 3) * K1_SLOT, kk = (ks) & 3; \
            xf[(ks) & 1] = ldsr128(xr + so + (((kk * 2 + hh) ^ sw) * 16)); \
            TRFRAG(wf[(ks) & 1][0], wqk + so + kk * 2048); TRFRAG(wf[(ks) & 1][1], wqk + 8192 + so + kk * 2048); } while (0)
        BAR();
        STAMP(0, 2);
        K1A_RD(0);
#pragma unroll
        for (int ks = 0; ks < 28; ++ks) {
            if ((ks & 3) == 3 && ks < 27) BAR();
            if (ks < 27) K1A_RD(ks + 1);
            SBAR();
            acc[0] = MFMA(wf[ks & 1][0], xf[ks & 1], acc[0]);
            acc[1] = MFMA(wf[ks & 1][1], xf[ks & 1], acc[1]);
            SBAR();
        }
        STAMP(0, 4);
        const float qs = 0.18898223650461362f * 1.4426950408889634f;
        const int tt = mb * 4 + tg;
#pragma unroll
        for (int r = 0; r < 16; ++r) acc[0][r] *= qs;
        if (hh) {
#pragma unroll
            for (int r = 12; r < 16; ++r) { acc[0][r] = 0.f; acc[1][r] = (r < 15) ? 1.f : 0.f; }
        }
        char* qo = ws + WS_QF + (size_t)(h * 64 + tt) * 2048 + lane * 16;
        char* kvo = ws + WS_KVF + (size_t)(h * 64 + tt) * 4096 + lane * 16;
        st16(qo, pack8(acc[0], 0)); st16(qo + 1024, pack8(acc[0], 1));
        st16(kvo, pack8(acc[1], 0)); st16(kvo + 1024, pack8(acc[1], 1));
        STAMP(0, 5);
    } else if (idx < 24) {
        const int vb = (xcd >> 1) * 2 + ((idx >> 2) & 1);
        if (wid >= 8) {
            __builtin_amdgcn_s_setprio(3);
            const char* xs = (const char*)(xA + (size_t)(vb * 256 + ld * 128) * DM);
            const char* wsrc = (const char*)(wqkv + (size_t)(ld * 32) * 1344 + hp * 56 + 896);
#define K1V_DMA(c) do { const unsigned sl = lds0 + ((c) & 3) * K1_SLOT; \
                _Pragma("unroll") for (int i = 0; i < 4; ++i) \
                    glds(woff, wsrc + (size_t)(CC(c) * 64 + i * 8) * 2688, sl + 32768 + ld * 4096 + i * 1024); \
                _Pragma("unroll") for (int i = 0; i < 8; ++i) { \
                    glds(xoff0, xs + i * 16 * DM * 2 + CC(c) * 128, sl + ld * 16384 + i * 2048); \
                    glds(xoff1, xs + i * 16 * DM * 2 + CC(c) * 128, sl + ld * 16384 + i * 2048 + 1024); } } while (0)
            K1_LOADER(K1V_DMA, 20, 40);
            return;
        }
        f32x16 acc[2];
        acc[0] = f32x16{}; acc[1] = f32x16{};
        const lds_cptr wv0 = (lds_cptr)lds + 32768 + wrow * 128 + ((120 + wcol + wrot) & 127);
        const lds_cptr wv1 = (lds_cptr)lds + 32768 + wrow * 128 + ((56 + 120 + wcol + wrot) & 127);
        const lds_cptr xr = (lds_cptr)lds + (wid * 32 + l31) * 128;
        bf16x8 xf[2], wf[2][2];
#define K1V_RD(ks) do { const int so = (((ks) >> 2) & 3) * K1_SLOT, kk = (ks) & 3; \
            xf[(ks) & 1] = ldsr128(xr + so + (((kk * 2 + hh) ^ sw) * 16)); \
            TRFRAG(wf[(ks) & 1][0], wv0 + so + kk * 2048); TRFRAG(wf[(ks) & 1][1], wv1 + so + kk * 2048); } while (0)
        BAR();
        STAMP(0, 2);
        K1V_RD(0);
#pragma unroll
        for (int ks = 0; ks < 28; ++ks) {
            if ((ks & 3) == 3 && ks < 27) BAR();
            if (ks < 27) K1V_RD(ks + 1);
            SBAR();
            acc[0] = MFMA(xf[ks & 1], wf[ks & 1][0], acc[0]);
            acc[1] = MFMA(xf[ks & 1], wf[ks & 1][1], acc[1]);
            SBAR();
        }
        STAMP(0, 4);
#pragma unroll
        for (int hs = 0; hs < 2; ++hs) {
            if (l31 < 4) {
#pragma unroll
                for (int r = 0; r < 16; ++r) acc[hs][r] = (l31 == 0) ? 1.f : 0.f;
            }
            char* kvo = ws + WS_KVF + (size_t)((hp * 2 + hs) * 64 + vb * 8 + wid) * 4096 + 2048 + lane * 16;
            st16(kvo, pack8(acc[hs], 0)); st16(kvo + 1024, pack8(acc[hs], 1));
        }
    } else if (idx < 31) {
        const int cp = idx - 24, sb = xcd;
        if (wid >= 8) {
            __builtin_amdgcn_s_setprio(3);
            const int e = cp * 2 + ld;
            const char* xs = (const char*)(xB + (size_t)(sb * 256 + ld * 128) * DM);
            const char* dsrc = (const char*)((e >= 7 ? dim : dre) + (size_t)((e >= 7 ? e - 7 : e) * 64) * DM);
#define K1B_DMA(c) do { const unsigned sl = lds0 + ((c) % 3) * 49152; \
                _Pragma("unroll") for (int i = 0; i < 4; ++i) { \
                    glds(xoff0, dsrc + i * 16 * DM * 2 + CC(c) * 128, sl + 32768 + ld * 8192 + i * 2048); \
                    glds(xoff1, dsrc + i * 16 * DM * 2 + CC(c) * 128, sl + 32768 + ld * 8192 + i * 2048 + 1024); } \
                _Pragma("unroll") for (int i = 0; i < 8; ++i) { \
                    glds(xoff0, xs + i * 16 * DM * 2 + CC(c) * 128, sl + ld * 16384 + i * 2048); \
                    glds(xoff1, xs + i * 16 * DM * 2 + CC(c) * 128, sl + ld * 16384 + i * 2048 + 1024); } } while (0)
            K1B_DMA(0); K1B_DMA(1); WAITBAR(24); K1B_DMA(2); WAITBAR(24); K1B_DMA(3); WAITBAR(24); K1B_DMA(4); WAITBAR(24);
            K1B_DMA(5); WAITBAR(24); K1B_DMA(6); WAITBAR(24); WAITBAR(0);
            return;
        }
        f32x16 acc[4];
#pragma unroll
        for (int t = 0; t < 4; ++t) acc[t] = f32x16{};
        const lds_cptr xr = (lds_cptr)lds + (wid * 32 + l31) * 128;
        const lds_cptr dr = (lds_cptr)lds + 32768 + l31 * 128;
        bf16x8 xf[2], df[2][4];
#define K1B_RD(ks) do { const int so = (((ks) >> 2) % 3) * 49152, co = ((((ks) & 3) * 2 + hh) ^ sw) * 16; \
            xf[(ks) & 1] = ldsr128(xr + so + co); \
            _Pragma("unroll") for (int t = 0; t < 4; ++t) df[(ks) & 1][t] = ldsr128(dr + so + t * 4096 + co); } while (0)
        BAR();
        STAMP(0, 2);
        K1B_RD(0);
#pragma unroll
        for (int ks = 0; ks < 28; ++ks) {
            if ((ks & 3) == 3 && ks < 27) { SBAR(); asm volatile("s_waitcnt lgkmcnt(0)\n\ts_barrier" ::: "memory"); SBAR(); }
            if (ks < 27) K1B_RD(ks + 1);
            SBAR();
#pragma unroll
            for (int t = 0; t < 4; ++t) acc[t] = MFMA(xf[ks & 1], df[ks & 1][t], acc[t]);
            SBAR();
        }
        STAMP(0, 4);
#pragma unroll
        for (int t = 0; t < 4; ++t) {
            const int e = cp * 2 + (t >> 1), im = e >= 7, et = im ? e - 7 : e;
            const int ng = et * 2 + (t & 1), nhalf = ng / 7, n7 = ng % 7;
            const float sg = im ? -1.f : 1.f;
            const int ks0 = im * 128 + sb * 16 + wid * 2;
#pragma unroll
            for (int r = 0; r < 16; ++r) acc[t][r] *= sg;
#pragma unroll
            for (int s = 0; s < 2; ++s)
                st16(ws + WS_ABF + ((size_t)(nhalf * 256 + ks0 + s) * 7 + n7) * 1024 + lane * 16, pack8n(acc[t], s));
        }
        STAMP(0, 5);
    } else if (wid < 8) {
        const int w0 = xcd * 8 + wid;
        u16 v[7][8];
#pragma unroll
        for (int i = 0; i < 7; ++i) {
            const int f = w0 + i * 64;
            const int nt = f >> 5, ks = f & 31, h = ks >> 1, s = ks & 1;
#pragma unroll
            for (int j = 0; j < 8; ++j) {
                const int rho = 16 * s + 8 * (j >> 2) + 4 * hh + (j & 3);
                const u16 t = wo[(size_t)(h * 28 + max(rho - 4, 0)) * DM + nt * 32 + l31];
                v[i][j] = (rho >= 4) ? t : (u16)0;
            }
        }
#pragma unroll
        for (int i = 0; i < 7; ++i) {
            const int f = w0 + i * 64;
            u32x4 o;
#pragma unroll
            for (int j = 0; j < 4; ++j) o[j] = (unsigned)v[i][2 * j] | ((unsigned)v[i][2 * j + 1] << 16);
            *(u32x4*)(ws + WS_WOF + (size_t)f * 1024 + lane * 16) = o;
        }
    } else {
#define PF_CODE 1
        unsigned long long c0, c1;
        asm volatile("s_getpc_b64 s[40:41]\n\ts_add_u32 s40, s40, _Z6k3_outPcPt@rel32@lo+4\n\ts_addc_u32 s41, s41, _Z6k3_outPcPt@rel32@hi+12\n\ts_mov_b64 %0, s[40:41]" : "=s"(c0) :: "scc", "s40", "s41");
        asm volatile("s_getpc_b64 s[40:41]\n\ts_add_u32 s40, s40, _Z7k1_projPKtS0_S0_S0_S0_S0_Pc@rel32@lo+4\n\ts_addc_u32 s41, s41, _Z7k1_projPKtS0_S0_S0_S0_S0_Pc@rel32@hi+12\n\ts_mov_b64 %0, s[40:41]" : "=s"(c1) :: "scc", "s40", "s41");
        unsigned dummy = 0;
#pragma unroll
        for (int i = 0; i < 3; ++i) {
            const unsigned long long a = c0 + (unsigned)((i * 128 + ld * 64 + lane) * 64);
            if (a < c1) asm volatile("global_load_dword %0, %1, off" : "+v"(dummy) : "v"(a) : "memory");
        }
        asm volatile("s_waitcnt vmcnt(0)" : "+v"(dummy) :: "memory");
    }
}

extern "C" void kernel_launch(void* const* d_in, const int* in_sizes, int n_in, void* d_out, int out_size, void* d_ws,
                              size_t ws_size, hipStream_t stream) {
    const u16* xA = (const u16*)d_in[0];
    const u16* wqkv = (const u16*)d_in[1];
    const u16* wo = (const u16*)d_in[2];
    const u16* xB = (const u16*)d_in[3];
    const u16* cre = (const u16*)d_in[4];
    const u16* cim = (const u16*)d_in[5];
    const u16* dre = (const u16*)d_in[6];
    const u16* dim = (const u16*)d_in[7];
    char* ws = (char*)d_ws;
    (void)hipFuncSetAttribute((const void*)k1_proj, hipFuncAttributeMaxDynamicSharedMemorySize, K1_LDS);
    (void)hipFuncSetAttribute((const void*)k2_attn_dft, hipFuncAttributeMaxDynamicSharedMemorySize, K2_LDS);
    k1_proj<<<256, 640, K1_LDS, stream>>>(xA, wqkv, wo, xB, dre, dim, ws);
    k2_attn_dft<<<256, 640, K2_LDS, stream>>>(cre, cim, ws);
    k3_out<<<448, 256, 0, stream>>>(ws, (u16*)d_out);
}
```

```cpp
#include <hip/hip_runtime.h>
#include <stdint.h>
#define ATT_UNROLL 4

typedef __attribute__((ext_vector_type(8))) short bf16x8;
typedef __attribute__((ext_vector_type(4))) short s16x4;
typedef __attribute__((ext_vector_type(16))) float f32x16;
typedef __attribute__((ext_vector_type(4))) unsigned u32x4;
typedef __attribute__((ext_vector_type(2))) unsigned u32x2;
typedef __attribute__((ext_vector_type(2))) float f32x2;
typedef __attribute__((ext_vector_type(4))) float f32x4;
typedef __attribute__((ext_vector_type(2))) __bf16 bf16x2_t;
typedef unsigned short u16;
typedef __attribute__((address_space(3))) const char* lds_cptr;
typedef short v4i16_t __attribute__((ext_vector_type(4)));

#define MFMA(a, b, c) __builtin_amdgcn_mfma_f32_32x32x16_bf16(a, b, c, 0, 0, 0)
#define ATT_NT 4
#define GEMM_N 7
#define NO_BAR 0
#define SBAR() __builtin_amdgcn_sched_barrier(0)

constexpr int SEQ = 2048, DM = 448;
#define STAMP(k, i)
constexpr size_t WS_QF = 0;
constexpr size_t WS_KVF = WS_QF + 2097152;
constexpr size_t WS_ABF = WS_KVF + 4194304;
constexpr size_t WS_WOF = WS_ABF + 3670016;
constexpr size_t WS_OF = WS_WOF + 458752;
constexpr size_t WS_YP = WS_OF + 2097152;

__device__ __forceinline__ unsigned cvtpk(float lo, float hi) {
    f32x2 v = {lo, hi};
    return __builtin_bit_cast(unsigned, __builtin_convertvector(v, bf16x2_t));
}
__device__ __forceinline__ void glds(unsigned voff, const void* sbase, unsigned lds_base) {
    asm volatile("s_mov_b32 m0, %2\n\ts_nop 0\n\tglobal_load_lds_dwordx4 %0, %1" ::"v"(voff), "s"(sbase), "s"(lds_base) : "memory");
}
__device__ __forceinline__ void glds4(unsigned voff, const void* sbase, unsigned lds_base) {
    asm volatile("s_mov_b32 m0, %2\n\ts_nop 0\n\tglobal_load_lds_dwordx4 %0, %1\n\tglobal_load_lds_dwordx4 %0, %1 offset:1024\n\t"
                 "global_load_lds_dwordx4 %0, %1 offset:2048\n\tglobal_load_lds_dwordx4 %0, %1 offset:3072"
                 ::"v"(voff), "s"(sbase), "s"(lds_base) : "memory");
}
__device__ __forceinline__ void glds3(unsigned voff, const void* sbase, unsigned lds_base) {
    asm volatile("s_mov_b32 m0, %2\n\ts_nop 0\n\tglobal_load_lds_dwordx4 %0, %1\n\tglobal_load_lds_dwordx4 %0, %1 offset:1024\n\t"
                 "global_load_lds_dwordx4 %0, %1 offset:2048"
                 ::"v"(voff), "s"(sbase), "s"(lds_base) : "memory");
}
__device__ __forceinline__ void gload16(bf16x8& d, const void* g) {
    asm volatile("global_load_dwordx4 %0, %1, off" : "=v"(d) : "v"(g) : "memory");
}
__device__ __forceinline__ bf16x8 ldsr128(lds_cptr p) { return *(const __attribute__((address_space(3))) bf16x8*)p; }
__device__ __forceinline__ s16x4 vtr(lds_cptr p) {
    return __builtin_bit_cast(s16x4, __builtin_amdgcn_ds_read_tr16_b64_v4i16((__attribute__((address_space(3))) v4i16_t*)p));
}
__device__ __forceinline__ u32x4 pack8(const f32x16& a, int s) {
    u32x4 r;
    r[0] = cvtpk(a[8 * s + 0], a[8 * s + 1]); r[1] = cvtpk(a[8 * s + 2], a[8 * s + 3]);
    r[2] = cvtpk(a[8 * s + 4], a[8 * s + 5]); r[3] = cvtpk(a[8 * s + 6], a[8 * s + 7]);
    return r;
}
__device__ __forceinline__ u32x4 pack8n(const f32x16& a, int s) {
    u32x4 p = pack8(a, s);
    auto x = __builtin_amdgcn_permlane32_swap(p[0], p[2], false, false);
    auto y = __builtin_amdgcn_permlane32_swap(p[1], p[3], false, false);
    u32x4 r; r[0] = x[0]; r[1] = y[0]; r[2] = x[1]; r[3] = y[1];
    return r;
}
#define ST_AUX 1
__device__ __forceinline__ void st16(char* p, u32x4 v) {
    asm volatile("global_store_dwordx4 %0, %1, off sc0 sc1\n\ts_nop 1" ::"v"(p), "v"(v) : "memory");
}
#define WAITBAR(N) do { SBAR(); asm volatile("s_waitcnt vmcnt(" #N ")\n\ts_barrier" ::: "memory"); SBAR(); } while (0)
#define BAR() do { SBAR(); asm volatile("s_barrier" ::: "memory"); SBAR(); } while (0)

__device__ __forceinline__ float bflo(unsigned u) { return __uint_as_float(u << 16); }
__device__ __forceinline__ float bfhi(unsigned u) { return __uint_as_float(u & 0xffff0000u); }

__global__ __launch_bounds__(256) void k3_out(char* __restrict__ ws, u16* __restrict__ out) {
    __shared__ f32x4 red[4][3][4][64];
    const int tid = threadIdx.x, lane = tid & 63, l31 = lane & 31, hh = lane >> 5;
    const int wid = __builtin_amdgcn_readfirstlane(tid >> 6);
    const int b = blockIdx.x, xcd = b & 7, idx = b >> 3;
    STAMP(2, 0);
    if (idx < 28) {
        const int tb = xcd * 4 + (idx & 3), nb = idx >> 2;
        const char* ap = ws + WS_OF + (size_t)(tb * 2 * 32 + wid * 8) * 1024 + lane * 16;
        const char* bp = ws + WS_WOF + (size_t)(nb * 2 * 32 + wid * 8) * 1024 + lane * 16;
        bf16x8 af[2][8], bf[2][8];
#pragma unroll
        for (int k = 0; k < 8; ++k) {
            gload16(af[0][k], ap + k * 1024); gload16(bf[0][k], bp + k * 1024);
            gload16(af[1][k], ap + 32768 + k * 1024); gload16(bf[1][k], bp + 32768 + k * 1024);
        }
        f32x16 acc[4];
#pragma unroll
        for (int i = 0; i < 4; ++i) acc[i] = f32x16{};
#define K3_STEP(k, N) do { \
            asm volatile("s_waitcnt vmcnt(" #N ")" : "+v"(af[0][k]), "+v"(bf[0][k]), "+v"(af[1][k]), "+v"(bf[1][k]), \
                         "+v"(af[0][k + 1]), "+v"(bf[0][k + 1]), "+v"(af[1][k + 1]), "+v"(bf[1][k + 1]) :: "memory"); \
            _Pragma("unroll") for (int kk = k; kk < k + 2; ++kk) \
            _Pragma("unroll") for (int i = 0; i < 4; ++i) acc[i] = MFMA(bf[i & 1][kk], af[i >> 1][kk], acc[i]); } while (0)
        K3_STEP(0, 24); K3_STEP(2, 16); K3_STEP(4, 8); K3_STEP(6, 0);
        STAMP(2, 1);
#pragma unroll
        for (int i = 0; i < 4; ++i) {
            if (i != wid) {
                const int slot = (wid - i - 1) & 3;
#pragma unroll
                for (int g = 0; g < 4; ++g)
                    red[i][slot][g][lane] = (f32x4){acc[i][4 * g], acc[i][4 * g + 1], acc[i][4 * g + 2], acc[i][4 * g + 3]};
            }
        }
        __syncthreads();
        f32x16 f = (wid == 0) ? acc[0] : (wid == 1) ? acc[1] : (wid == 2) ? acc[2] : acc[3];
#pragma unroll
        for (int s = 0; s < 3; ++s)
#pragma unroll
            for (int g = 0; g < 4; ++g) {
                const f32x4 t = red[wid][s][g][lane];
#pragma unroll
                for (int j = 0; j < 4; ++j) f[4 * g + j] += t[j];
            }
        const int t = tb * 64 + (wid >> 1) * 32 + l31, n0 = nb * 64 + (wid & 1) * 32 + 8 * hh;
        *(u32x4*)(out + (size_t)t * 896 + n0) = pack8n(f, 0);
        *(u32x4*)(out + (size_t)t * 896 + n0 + 16) = pack8n(f, 1);
        STAMP(2, 2);
    } else {
        const int task = ((idx - 28) * 8 + xcd) * 4 + wid;
        const int tt = task / 14, ng = task % 14;
        const char* yp = ws + WS_YP + ((size_t)tt * 14 + ng) * 2048 + lane * 16;
        u32x4 v[8][2];
#pragma unroll
        for (int s = 0; s < 8; ++s) {
            v[s][0] = *(const u32x4*)(yp + (size_t)s * 64 * 14 * 2048);
            v[s][1] = *(const u32x4*)(yp + (size_t)s * 64 * 14 * 2048 + 1024);
        }
        const int t = tt * 32 + l31, n0 = 448 + ng * 32 + 8 * hh;
#pragma unroll
        for (int i = 0; i < 2; ++i) {
            u32x4 o;
#pragma unroll
            for (int j = 0; j < 4; ++j) {
                float lo = 0.f, hi = 0.f;
#pragma unroll
                for (int s = 0; s < 8; ++s) { lo += bflo(v[s][i][j]); hi += bfhi(v[s][i][j]); }
                o[j] = cvtpk(lo, hi);
            }
            *(u32x4*)(out + (size_t)t * 896 + n0 + 16 * i) = o;
        }
        STAMP(2, 2);
    }
}

constexpr int K2_KV = 0, K2_B = 65536, K2_A = 122880, K2_LDS = 155648;
constexpr float ATT_T = 30.f;
constexpr float ATT_LIM = 1.1529215e18f;

__device__ __forceinline__ float bf_trunc(float x) { return __uint_as_float(__float_as_uint(x) & 0xffff0000u); }
__device__ __forceinline__ bf16x8 set_ref(bf16x8 qf1, float mref, int hh) {
    const float nm = -mref;
    const float m0 = bf_trunc(nm), r1 = nm - m0, m1 = bf_trunc(r1), m2 = bf_trunc(r1 - m1);
    u32x4 q1 = __builtin_bit_cast(u32x4, qf1);
    if (hh) {
        q1[2] = (__float_as_uint(m0) >> 16) | (__float_as_uint(m1) & 0xffff0000u);
        q1[3] = (__float_as_uint(m2) >> 16);
    }
    return __builtin_bit_cast(bf16x8, q1);
}
__device__ __forceinline__ float rowmax(const f32x16& s) {
    float mx = s[0];
#pragma unroll
    for (int r = 1; r < 16; ++r) mx = fmaxf(mx, s[r]);
    auto rr = __builtin_amdgcn_permlane32_swap(__float_as_uint(mx), __float_as_uint(mx), false, false);
    return fmaxf(__uint_as_float(rr[0]), __uint_as_float(rr[1]));
}
#define EXP4(S, i) do { S[i] = __builtin_amdgcn_exp2f(S[i]); S[i + 1] = __builtin_amdgcn_exp2f(S[i + 1]); \
                        S[i + 2] = __builtin_amdgcn_exp2f(S[i + 2]); S[i + 3] = __builtin_amdgcn_exp2f(S[i + 3]); } while (0)

__global__ __launch_bounds__(640) void k2_attn_dft(const u16* __restrict__ cre, const u16* __restrict__ cim,
                                                   char* __restrict__ ws) {
    extern __shared__ __attribute__((aligned(16))) char lds[];
    const int tid = threadIdx.x, lane = tid & 63, l31 = lane & 31, hh = lane >> 5;
    const int wid = __builtin_amdgcn_readfirstlane(tid >> 6);
    const int b = blockIdx.x, xcd = b & 7, idx = b >> 3;
    const unsigned lds0 = (unsigned)(uintptr_t)lds;
    const int h = xcd * 2 + (idx & 1);
    const int split = xcd, mt = idx >> 1, nhalf = idx & 1;
    STAMP(1, 0);
    if (wid >= 8) {
#define LD_PRIO 3
        __builtin_amdgcn_s_setprio(LD_PRIO);
        const char* bsrc = ws + WS_ABF + (size_t)(nhalf * 256 + split * 32) * 7168;
        const unsigned lo = lane * 16;
#define ROT2 0
#define SR(s) (ROT2 ? (((s) + mt) & 15) : (s))
        if (wid == 8) {
            const char* kvsrc = ws + WS_KVF + (size_t)h * 262144;
#define K2_L0(s) do { const unsigned sl = (s) & 3; \
                _Pragma("unroll") for (int i = 0; i < 4; ++i) glds4(lo, kvsrc + (size_t)(s) * 16384 + i * 4096, lds0 + K2_KV + sl * 16384 + i * 4096); \
                glds3(lo, bsrc + (size_t)SR(s) * 14336, lds0 + K2_B + sl * 14336); } while (0)
            K2_L0(0); K2_L0(1); WAITBAR(19); K2_L0(2);
            WAITBAR(19); K2_L0(3);
            for (int s = 4; s < 16; ++s) { if (!NO_BAR) WAITBAR(19);
 K2_L0(s);
 }
        } else {
            const char* cm = (const char*)((split < 4 ? cre : cim) + (size_t)(mt * 128) * SEQ + (split & 3) * 512);
            const unsigned ao = (unsigned)(lane >> 2) * 4096 + (unsigned)(((lane & 3) ^ ((lane >> 4) & 3)) * 16);
#define K2_L1(s) do { const unsigned sl = (s) & 3; \
                _Pragma("unroll") for (int i = 0; i < 8; ++i) glds(ao, cm + (size_t)i * 65536 + SR(s) * 64, lds0 + K2_A + sl * 8192 + i * 1024); \
                glds4(lo, bsrc + (size_t)SR(s) * 14336 + 3072, lds0 + K2_B + sl * 14336 + 3072); \
                glds4(lo, bsrc + (size_t)SR(s) * 14336 + 7168, lds0 + K2_B + sl * 14336 + 7168); \
                glds3(lo, bsrc + (size_t)SR(s) * 14336 + 11264, lds0 + K2_B + sl * 14336 + 11264); } while (0)
            K2_L1(0); K2_L1(1); WAITBAR(19); K2_L1(2);
            WAITBAR(19); K2_L1(3);
            for (int s = 4; s < 16; ++s) { if (!NO_BAR) WAITBAR(19);
 K2_L1(s);
 }
        }
        WAITBAR(19);
        WAITBAR(0);
        BAR();
    } else if (wid < 4) {
        const int qh = wid & 1, kh = wid >> 1;
        const int qt = (idx >> 1) * 4 + qh * 2;
        const char* qsrc = ws + WS_QF + (size_t)(h * 64 + qt) * 2048 + lane * 16;
        const bf16x8 qa0 = *(const bf16x8*)qsrc, qb0 = *(const bf16x8*)(qsrc + 2048);
        bf16x8 qa1 = *(const bf16x8*)(qsrc + 1024), qb1 = *(const bf16x8*)(qsrc + 3072);
        const f32x16 zero16 = f32x16{};
        const lds_cptr kv0 = (lds_cptr)lds + K2_KV + lane * 16;
        const lds_cptr kv = kv0 + kh * 4096;
        BAR();
        STAMP(1, 1);
        bf16x8 kA0 = ldsr128(kv0), kA1 = ldsr128(kv0 + 1024);
        f32x16 s = MFMA(kA0, qa0, zero16), sn = MFMA(kA0, qb0, zero16);
        s = MFMA(kA1, qa1, s); sn = MFMA(kA1, qb1, sn);
        kA0 = ldsr128(kv); kA1 = ldsr128(kv + 1024);
        bf16x8 vA0 = ldsr128(kv + 2048), vA1 = ldsr128(kv + 3072);
        bf16x8 kB0 = ldsr128(kv + 8192), kB1 = ldsr128(kv + 8192 + 1024), vB0, vB1;
        qa1 = set_ref(qa1, rowmax(s) + ATT_T, hh);
        qb1 = set_ref(qb1, rowmax(sn) + ATT_T, hh);
        s = MFMA(kA0, qa0, zero16);
        s = MFMA(kA1, qa1, s);
        f32x16 oa = f32x16{}, ob = f32x16{};
#define E1(S, i) S[i] = __builtin_amdgcn_exp2f(S[i])
#define UNIT(S, O, V0, V1, SN, K0, K1, Q0, Q1, L0, L1, LP) do { u32x4 p0, p1; \
            SBAR(); E1(S, 0); E1(S, 1); E1(S, 2); E1(S, 3); \
            SBAR(); SN = MFMA(K0, Q0, zero16); \
            SBAR(); E1(S, 4); E1(S, 5); p0[0] = cvtpk(S[0], S[1]); p0[1] = cvtpk(S[2], S[3]); E1(S, 6); E1(S, 7); \
            SBAR(); p0[2] = cvtpk(S[4], S[5]); p0[3] = cvtpk(S[6], S[7]); \
            SBAR(); E1(S, 8); L0 = ldsr128(LP); \
            SBAR(); O = MFMA(V0, __builtin_bit_cast(bf16x8, p0), O); \
            SBAR(); E1(S, 9); E1(S, 10); E1(S, 11); \
            SBAR(); SN = MFMA(K1, Q1, SN); \
            SBAR(); E1(S, 12); E1(S, 13); p1[0] = cvtpk(S[8], S[9]); p1[1] = cvtpk(S[10], S[11]); E1(S, 14); E1(S, 15); \
            SBAR(); p1[2] = cvtpk(S[12], S[13]); p1[3] = cvtpk(S[14], S[15]); \
            SBAR(); L1 = ldsr128(LP + 1024); \
            SBAR(); O = MFMA(V1, __builtin_bit_cast(bf16x8, p1), O); SBAR(); } while (0)
#pragma unroll ATT_UNROLL
        for (int st = 0; st < 16; ++st) {
            const lds_cptr cur = kv + (st & 3) * 16384, nxt = kv + ((st + 1) & 3) * 16384;
            UNIT(s, oa, vA0, vA1, sn, kA0, kA1, qb0, qb1, vB0, vB1, cur + 8192 + 2048);
            if (st < 15 && (!NO_BAR || st == 0 || st > 12)) BAR();
            UNIT(sn, ob, vA0, vA1, s, kB0, kB1, qa0, qa1, kA0, kA1, nxt);
            UNIT(s, oa, vB0, vB1, sn, kB0, kB1, qb0, qb1, vA0, vA1, nxt + 2048);
            UNIT(sn, ob, vB0, vB1, s, kA0, kA1, qa0, qa1, kB0, kB1, nxt + 8192);
            if (st == 7) STAMP(1, 2);
        }
        STAMP(1, 3);
        f32x16 o;
        {
            char* xb = lds + K2_KV + 16384 + lane * 16;
#pragma unroll
            for (int g = 0; g < 4; ++g) {
                f32x4 t;
#pragma unroll
                for (int j = 0; j < 4; ++j) t[j] = kh ? oa[4 * g + j] : ob[4 * g + j];
                *(f32x4*)(xb + (wid ^ 2) * 4096 + g * 1024) = t;
            }
            asm volatile("s_waitcnt lgkmcnt(0)\n\ts_barrier" ::: "memory");
#pragma unroll
            for (int g = 0; g < 4; ++g) {
                const f32x4 t = *(const f32x4*)(xb + wid * 4096 + g * 1024);
#pragma unroll
                for (int j = 0; j < 4; ++j) o[4 * g + j] = (kh ? ob[4 * g + j] : oa[4 * g + j]) + t[j];
            }
        }
        {
            auto rr = __builtin_amdgcn_permlane32_swap(__float_as_uint(o[0]), __float_as_uint(o[0]), false, false);
            const float l = __uint_as_float(rr[0]);
            if (__builtin_expect(__any(!(l < ATT_LIM)), 0)) {
                const char* kvg = ws + WS_KVF + (size_t)h * 262144 + lane * 16;
                const bf16x8 qf0 = kh ? qb0 : qa0;
                bf16x8 qf1 = set_ref(kh ? qb1 : qa1, 0.f, hh);
                float mref = 0.f;
                o = zero16;
                for (int t = 0; t < 64; ++t) {
                    kA0 = *(const bf16x8*)(kvg + t * 4096); kA1 = *(const bf16x8*)(kvg + t * 4096 + 1024);
                    vA0 = *(const bf16x8*)(kvg + t * 4096 + 2048); vA1 = *(const bf16x8*)(kvg + t * 4096 + 3072);
                    s = MFMA(kA0, qf0, zero16);
                    s = MFMA(kA1, qf1, s);
                    float dl = rowmax(s);
                    if (t) dl = fmaxf(dl, 0.f);
                    const float mnew = mref + dl;
                    const float d2 = mnew - mref;
                    const float alpha = t ? __builtin_amdgcn_exp2f(-d2) : 1.f;
                    mref = mnew;
                    qf1 = set_ref(qf1, mref, hh);
#pragma unroll
                    for (int r = 0; r < 16; ++r) { s[r] = __builtin_amdgcn_exp2f(s[r] - d2); o[r] *= alpha; }
                    u32x4 p0 = pack8(s, 0), p1 = pack8(s, 1);
                    o = MFMA(vA0, __builtin_bit_cast(bf16x8, p0), o);
                    o = MFMA(vA1, __builtin_bit_cast(bf16x8, p1), o);
                }
            }
        }
        auto rr = __builtin_amdgcn_permlane32_swap(__float_as_uint(o[0]), __float_as_uint(o[0]), false, false);
        const float linv = 1.f / __uint_as_float(rr[0]);
#pragma unroll
        for (int r = 0; r < 16; ++r) o[r] *= linv;
        char* oo = ws + WS_OF + (size_t)((qt + kh) * 32 + h * 2) * 1024 + lane * 16;
        st16(oo, pack8(o, 0));
        st16(oo + 1024, pack8(o, 1));
        STAMP(1, 4);
    } else {
        const int w = wid - 4;
        f32x16 acc[7];
#pragma unroll
        for (int n = 0; n < 7; ++n) acc[n] = f32x16{};
        const int asw = (l31 >> 2) & 3;
        const lds_cptr a0 = (lds_cptr)lds + K2_A + (w * 32 + l31) * 64 + ((hh ^ asw) * 16);
        const lds_cptr a1 = (lds_cptr)lds + K2_A + (w * 32 + l31) * 64 + (((2 + hh) ^ asw) * 16);
        const lds_cptr bb = (lds_cptr)lds + K2_B + lane * 16;
        BAR();
        STAMP(1, 1);
        bf16x8 bq[7], af, an;
        af = ldsr128(a0);
#pragma unroll
        for (int n = 0; n < 7; ++n) bq[n] = ldsr128(bb + n * 1024);
        for (int st = 0; st < 16; ++st) {
            const int sl = st & 3, sn = (st + 1) & 3;
            const lds_cptr bc = bb + sl * 14336 + 7168, bn = bb + sn * 14336;
            an = ldsr128(a1 + sl * 8192);
#pragma unroll
            for (int n = 0; n < GEMM_N; ++n) {
                SBAR();
                acc[n] = MFMA(bq[n], af, acc[n]);
                bq[n] = ldsr128(bc + n * 1024);
            }
            if (st < 15 && (!NO_BAR || st == 0 || st > 12)) BAR();
            af = ldsr128(a0 + sn * 8192);
#pragma unroll
            for (int n = 0; n < GEMM_N; ++n) {
                SBAR();
                acc[n] = MFMA(bq[n], an, acc[n]);
                bq[n] = ldsr128(bn + n * 1024);
            }
        }
        STAMP(1, 3);
        char* yo = ws + WS_YP + ((size_t)(split * 64 + mt * 4 + w) * 14 + nhalf * 7) * 2048 + lane * 16;
#pragma unroll
        for (int n = 0; n < 7; ++n) {
            st16(yo + n * 2048, pack8n(acc[n], 0));
            st16(yo + n * 2048 + 1024, pack8n(acc[n], 1));
        }
        BAR();
        STAMP(1, 4);
    }
}

constexpr int K1_SLOT = 40960, K1_LDS = 4 * K1_SLOT;

#define K1_LOADER(DMA) do { \
        DMA(0); DMA(1); WAITBAR(20); DMA(2); DMA(3); WAITBAR(40); \
        WAITBAR(20); DMA(4); WAITBAR(20); DMA(5); WAITBAR(20); DMA(6); WAITBAR(20); WAITBAR(0); } while (0)

__global__ __launch_bounds__(640) void k1_proj(const u16* __restrict__ xA, const u16* __restrict__ wqkv,
                                               const u16* __restrict__ wo, const u16* __restrict__ xB,
                                               const u16* __restrict__ dre, const u16* __restrict__ dim,
                                               const u16* __restrict__ cre, const u16* __restrict__ cim,
                                               char* __restrict__ ws) {
    extern __shared__ __attribute__((aligned(16))) char lds[];
    const int tid = threadIdx.x, lane = tid & 63, l31 = lane & 31, hh = lane >> 5;
    const int wid = __builtin_amdgcn_readfirstlane(tid >> 6);
    const int b = blockIdx.x, xcd = b & 7, idx = b >> 3;
    const unsigned lds0 = (unsigned)(uintptr_t)lds;
    const unsigned xoff0 = (unsigned)(lane >> 3) * (DM * 2) + (unsigned)(((lane & 7) ^ (lane >> 4)) * 16);
    const unsigned xoff1 = (unsigned)(lane >> 3) * (DM * 2) + (unsigned)(((lane & 7) ^ (4 + (lane >> 4))) * 16) + 8 * DM * 2;
    const int sw = (l31 >> 1) & 7;
    const int ld = wid - 8;
#define ROTX 1
#define ROT1 1
    const int rot = (idx * ROT1 + xcd * ROTX) % 7;
#define CC(c) ((c) + rot >= 7 ? (c) + rot - 7 : (c) + rot)
    STAMP(0, 0);
    if (idx < 16) {
        const int hp = (xcd & 1) * 4 + (idx & 3), mb = (xcd >> 1) * 4 + (idx >> 2);
        if (wid >= 8) {
            __builtin_amdgcn_s_setprio(3);
            const char* xs = (const char*)(xA + (size_t)(mb * 128 + ld * 64) * DM);
            const char* wsrc = (const char*)(wqkv + (size_t)(ld * 32) * 1344 + hp * 56);
            int wc = (lane & 7) ^ (((lane >> 4) & 1) * 4); wc = wc > 6 ? 6 : wc;
            const unsigned woff = (unsigned)(lane >> 3) * 2688 + (unsigned)wc * 16;
#define K1A_DMA(c) do { const unsigned sl = lds0 + ((c) & 3) * K1_SLOT; \
                _Pragma("unroll") for (int i = 0; i < 4; ++i) { \
                    glds(xoff0, xs + i * 16 * DM * 2 + CC(c) * 128, sl + ld * 8192 + i * 2048); \
                    glds(xoff1, xs + i * 16 * DM * 2 + CC(c) * 128, sl + ld * 8192 + i * 2048 + 1024); \
                    _Pragma("unroll") for (int g = 0; g < 3; ++g) \
                        glds(woff, wsrc + (size_t)(CC(c) * 64 + i * 8) * 2688 + g * 896, sl + 16384 + g * 8192 + ld * 4096 + i * 1024); } } while (0)
            K1_LOADER(K1A_DMA);
            return;
        }
        const int tg = wid & 3, hs = wid >> 2, h = hp * 2 + hs;
        f32x16 acc[3];
#pragma unroll
        for (int g = 0; g < 3; ++g) acc[g] = f32x16{};
        const int wrow = 8 * hh + ((lane & 15) >> 2), wrot = ((lane >> 3) & 1) * 64, wcol = ((lane >> 4) & 1) * 32 + (lane & 3) * 8;
        const lds_cptr wqk = (lds_cptr)lds + 16384 + wrow * 128 + ((hs * 56 + wcol + wrot) & 127);
        const lds_cptr wv = (lds_cptr)lds + 16384 + 16384 + wrow * 128 + ((hs * 56 + 120 + wcol + wrot) & 127);
        const lds_cptr xr = (lds_cptr)lds + (tg * 32 + l31) * 128;
        bf16x8 xf[2], wf[2][3];
#define K1A_RD(ks) do { const int so = (((ks) >> 2) & 3) * K1_SLOT, kk = (ks) & 3; \
            xf[(ks) & 1] = ldsr128(xr + so + (((kk * 2 + hh) ^ sw) * 16)); \
            _Pragma("unroll") for (int g = 0; g < 3; ++g) { \
                lds_cptr p = (g < 2 ? wqk + g * 8192 : wv) + so + kk * 2048; \
                s16x4 lo = vtr(p), hi = vtr(p + 512); \
                wf[(ks) & 1][g] = (bf16x8){lo[0], lo[1], lo[2], lo[3], hi[0], hi[1], hi[2], hi[3]}; } } while (0)
        BAR();
        STAMP(0, 2);
        K1A_RD(0);
#pragma unroll
        for (int ks = 0; ks < 28; ++ks) {
            if ((ks & 3) == 3 && ks < 27) BAR();
            if (ks < 27) K1A_RD(ks + 1);
            SBAR();
            acc[0] = MFMA(wf[ks & 1][0], xf[ks & 1], acc[0]);
            acc[1] = MFMA(wf[ks & 1][1], xf[ks & 1], acc[1]);
            acc[2] = MFMA(xf[ks & 1], wf[ks & 1][2], acc[2]);
            SBAR();
        }
        STAMP(0, 4);
        const float qs = 0.18898223650461362f * 1.4426950408889634f;
        const int tt = mb * 4 + tg;
#pragma unroll
        for (int r = 0; r < 16; ++r) acc[0][r] *= qs;
        if (hh) {
#pragma unroll
            for (int r = 12; r < 16; ++r) { acc[0][r] = 0.f; acc[1][r] = (r < 15) ? 1.f : 0.f; }
        }
        if (l31 < 4) {
#pragma unroll
            for (int r = 0; r < 16; ++r) acc[2][r] = (l31 == 0) ? 1.f : 0.f;
        }
        char* qo = ws + WS_QF + (size_t)(h * 64 + tt) * 2048 + lane * 16;
        char* kvo = ws + WS_KVF + (size_t)(h * 64 + tt) * 4096 + lane * 16;
        st16(qo, pack8(acc[0], 0)); st16(qo + 1024, pack8(acc[0], 1));
        st16(kvo, pack8(acc[1], 0)); st16(kvo + 1024, pack8(acc[1], 1));
        st16(kvo + 2048, pack8(acc[2], 0)); st16(kvo + 3072, pack8(acc[2], 1));
        STAMP(0, 5);
    } else if (idx < 30) {
        const int j = idx - 16, im = xcd >> 2;
        const int sb = (xcd & 3) * 2 + (j & 1), et = j >> 1;
        if (wid >= 8) {
            __builtin_amdgcn_s_setprio(3);
            const char* xs = (const char*)(xB + (size_t)(sb * 256 + ld * 128) * DM);
            const char* dsrc = (const char*)((im ? dim : dre) + (size_t)(et * 64 + ld * 32) * DM);
#define K1B_DMA(c) do { const unsigned sl = lds0 + ((c) & 3) * K1_SLOT; \
                _Pragma("unroll") for (int i = 0; i < 2; ++i) { \
                    glds(xoff0, dsrc + i * 16 * DM * 2 + CC(c) * 128, sl + 32768 + ld * 4096 + i * 2048); \
                    glds(xoff1, dsrc + i * 16 * DM * 2 + CC(c) * 128, sl + 32768 + ld * 4096 + i * 2048 + 1024); } \
                _Pragma("unroll") for (int i = 0; i < 8; ++i) { \
                    glds(xoff0, xs + i * 16 * DM * 2 + CC(c) * 128, sl + ld * 16384 + i * 2048); \
                    glds(xoff1, xs + i * 16 * DM * 2 + CC(c) * 128, sl + ld * 16384 + i * 2048 + 1024); } } while (0)
            K1_LOADER(K1B_DMA);
#define PF_A 2
            {
                unsigned dummy = 0;
#pragma unroll
                for (int i = 0; i < 2; ++i) {
                    const int q = ld * 64 + lane + i * 128, r = j * 147 + q;
                    const char* a = (const char*)((xcd < 4 ? cre : cim) + (size_t)r * SEQ + (xcd & 3) * 512);
                    if (q < 147 && r < SEQ) {
#pragma unroll
                        for (int l = 0; l < PF_A; ++l)
                            asm volatile("global_load_dword %0, %1, off" : "+v"(dummy) : "v"(a + l * 128) : "memory");
                    }
                }
                asm volatile("s_waitcnt vmcnt(0)" : "+v"(dummy) :: "memory");
            }
            return;
        }
        f32x16 acc[2];
        acc[0] = f32x16{}; acc[1] = f32x16{};
        const lds_cptr xr = (lds_cptr)lds + (wid * 32 + l31) * 128;
        const lds_cptr dr = (lds_cptr)lds + 32768 + l31 * 128;
        bf16x8 xf[2], df[2][2];
#define K1B_RD(ks) do { const int so = (((ks) >> 2) & 3) * K1_SLOT, co = ((((ks) & 3) * 2 + hh) ^ sw) * 16; \
            xf[(ks) & 1] = ldsr128(xr + so + co); df[(ks) & 1][0] = ldsr128(dr + so + co); df[(ks) & 1][1] = ldsr128(dr + so + 4096 + co); } while (0)
        BAR();
        STAMP(0, 2);
        K1B_RD(0);
#pragma unroll
        for (int ks = 0; ks < 28; ++ks) {
            if ((ks & 3) == 3 && ks < 27) BAR();
            if (ks < 27) K1B_RD(ks + 1);
            SBAR();
            acc[0] = MFMA(xf[ks & 1], df[ks & 1][0], acc[0]);
            acc[1] = MFMA(xf[ks & 1], df[ks & 1][1], acc[1]);
            SBAR();
        }
        STAMP(0, 4);
        const float sg = im ? -1.f : 1.f;
        const int ks0 = im * 128 + sb * 16 + wid * 2;
#pragma unroll
        for (int nt = 0; nt < 2; ++nt) {
            const int ng = et * 2 + nt, nhalf = ng / 7, n7 = ng % 7;
#pragma unroll
            for (int r = 0; r < 16; ++r) acc[nt][r] *= sg;
#pragma unroll
            for (int s = 0; s < 2; ++s)
                st16(ws + WS_ABF + ((size_t)(nhalf * 256 + ks0 + s) * 7 + n7) * 1024 + lane * 16, pack8n(acc[nt], s));
        }
        STAMP(0, 5);
    } else if (wid < 8) {
        const int w0 = (xcd * 2 + (idx - 30)) * 8 + wid;
        u16 v[4][8];
#pragma unroll
        for (int i = 0; i < 4; ++i) {
            const int f = min(w0 + i * 128, 447);
            const int nt = f >> 5, ks = f & 31, h = ks >> 1, s = ks & 1;
#pragma unroll
            for (int j = 0; j < 8; ++j) {
                const int rho = 16 * s + 8 * (j >> 2) + 4 * hh + (j & 3);
                const u16 t = wo[(size_t)(h * 28 + max(rho - 4, 0)) * DM + nt * 32 + l31];
                v[i][j] = (rho >= 4) ? t : (u16)0;
            }
        }
#pragma unroll
        for (int i = 0; i < 4; ++i) {
            const int f = min(w0 + i * 128, 447);
            u32x4 o;
#pragma unroll
            for (int j = 0; j < 4; ++j) o[j] = (unsigned)v[i][2 * j] | ((unsigned)v[i][2 * j + 1] << 16);
            *(u32x4*)(ws + WS_WOF + (size_t)f * 1024 + lane * 16) = o;
        }
    } else if (idx == 30) {
#define PF_CODE 1
        unsigned long long c0, c1;
        asm volatile("s_getpc_b64 s[40:41]\n\ts_add_u32 s40, s40, _Z6k3_outPcPt@rel32@lo+4\n\ts_addc_u32 s41, s41, _Z6k3_outPcPt@rel32@hi+12\n\ts_mov_b64 %0, s[40:41]" : "=s"(c0) :: "scc", "s40", "s41");
        asm volatile("s_getpc_b64 s[40:41]\n\ts_add_u32 s40, s40, _Z7k1_projPKtS0_S0_S0_S0_S0_S0_S0_Pc@rel32@lo+4\n\ts_addc_u32 s41, s41, _Z7k1_projPKtS0_S0_S0_S0_S0_S0_S0_Pc@rel32@hi+12\n\ts_mov_b64 %0, s[40:41]" : "=s"(c1) :: "scc", "s40", "s41");
        unsigned dummy = 0;
#pragma unroll
        for (int i = 0; i < 5; ++i) {
            const unsigned long long a = c0 + (unsigned)((i * 128 + ld * 64 + lane) * 64);
            if (a < c1) asm volatile("global_load_dword %0, %1, off" : "+v"(dummy) : "v"(a) : "memory");
        }
        asm volatile("s_waitcnt vmcnt(0)" : "+v"(dummy) :: "memory");
    }
}

extern "C" void kernel_launch(void* const* d_in, const int* in_sizes, int n_in, void* d_out, int out_size, void* d_ws,
                              size_t ws_size, hipStream_t stream) {
    const u16* xA = (const u16*)d_in[0];
    const u16* wqkv = (const u16*)d_in[1];
    const u16* wo = (const u16*)d_in[2];
    const u16* xB = (const u16*)d_in[3];
    const u16* cre = (const u16*)d_in[4];
    const u16* cim = (const u16*)d_in[5];
    const u16* dre = (const u16*)d_in[6];
    const u16* dim = (const u16*)d_in[7];
    char* ws = (char*)d_ws;
    (void)hipFuncSetAttribute((const void*)k1_proj, hipFuncAttributeMaxDynamicSharedMemorySize, K1_LDS);
    (void)hipFuncSetAttribute((const void*)k2_attn_dft, hipFuncAttributeMaxDynamicSharedMemorySize, K2_LDS);
    k1_proj<<<256, 640, K1_LDS, stream>>>(xA, wqkv, wo, xB, dre, dim, cre, cim, ws);
    k2_attn_dft<<<256, 640, K2_LDS, stream>>>(cre, cim, ws);
    k3_out<<<448, 256, 0, stream>>>(ws, (u16*)d_out);
}
```

```cpp
#include <hip/hip_runtime.h>
#include <stdint.h>
#define ATT_UNROLL 4

typedef __attribute__((ext_vector_type(8))) short bf16x8;
typedef __attribute__((ext_vector_type(4))) short s16x4;
typedef __attribute__((ext_vector_type(16))) float f32x16;
typedef __attribute__((ext_vector_type(4))) unsigned u32x4;
typedef __attribute__((ext_vector_type(2))) unsigned u32x2;
typedef __attribute__((ext_vector_type(2))) float f32x2;
typedef __attribute__((ext_vector_type(4))) float f32x4;
typedef __attribute__((ext_vector_type(2))) __bf16 bf16x2_t;
typedef unsigned short u16;
typedef __attribute__((address_space(3))) const char* lds_cptr;
typedef short v4i16_t __attribute__((ext_vector_type(4)));

#define MFMA(a, b, c) __builtin_amdgcn_mfma_f32_32x32x16_bf16(a, b, c, 0, 0, 0)
#define ATT_NT 4
#define GEMM_N 7
#define NO_BAR 0
#define SBAR() __builtin_amdgcn_sched_barrier(0)

constexpr int SEQ = 2048, DM = 448;
constexpr size_t WS_QF = 0;
constexpr size_t WS_KVF = WS_QF + 2097152;
constexpr size_t WS_ABF = WS_KVF + 4194304;
constexpr size_t WS_WOF = WS_ABF + 3670016;
constexpr size_t WS_OF = WS_WOF + 458752;
constexpr size_t WS_YP = WS_OF + 2097152;

__device__ __forceinline__ unsigned cvtpk(float lo, float hi) {
    f32x2 v = {lo, hi};
    return __builtin_bit_cast(unsigned, __builtin_convertvector(v, bf16x2_t));
}
__device__ __forceinline__ void glds(unsigned voff, const void* sbase, unsigned lds_base) {
    asm volatile("s_mov_b32 m0, %2\n\ts_nop 0\n\tglobal_load_lds_dwordx4 %0, %1" ::"v"(voff), "s"(sbase), "s"(lds_base) : "memory");
}
__device__ __forceinline__ void glds4(unsigned voff, const void* sbase, unsigned lds_base) {
    asm volatile("s_mov_b32 m0, %2\n\ts_nop 0\n\tglobal_load_lds_dwordx4 %0, %1\n\tglobal_load_lds_dwordx4 %0, %1 offset:1024\n\t"
                 "global_load_lds_dwordx4 %0, %1 offset:2048\n\tglobal_load_lds_dwordx4 %0, %1 offset:3072"
                 ::"v"(voff), "s"(sbase), "s"(lds_base) : "memory");
}
__device__ __forceinline__ void glds3(unsigned voff, const void* sbase, unsigned lds_base) {
    asm volatile("s_mov_b32 m0, %2\n\ts_nop 0\n\tglobal_load_lds_dwordx4 %0, %1\n\tglobal_load_lds_dwordx4 %0, %1 offset:1024\n\t"
                 "global_load_lds_dwordx4 %0, %1 offset:2048"
                 ::"v"(voff), "s"(sbase), "s"(lds_base) : "memory");
}
__device__ __forceinline__ void gload16(bf16x8& d, const void* g) {
    asm volatile("global_load_dwordx4 %0, %1, off" : "=v"(d) : "v"(g) : "memory");
}
__device__ __forceinline__ bf16x8 ldsr128(lds_cptr p) { return *(const __attribute__((address_space(3))) bf16x8*)p; }
__device__ __forceinline__ s16x4 vtr(lds_cptr p) {
    return __builtin_bit_cast(s16x4, __builtin_amdgcn_ds_read_tr16_b64_v4i16((__attribute__((address_space(3))) v4i16_t*)p));
}
__device__ __forceinline__ u32x4 pack8(const f32x16& a, int s) {
    u32x4 r;
    r[0] = cvtpk(a[8 * s + 0], a[8 * s + 1]); r[1] = cvtpk(a[8 * s + 2], a[8 * s + 3]);
    r[2] = cvtpk(a[8 * s + 4], a[8 * s + 5]); r[3] = cvtpk(a[8 * s + 6], a[8 * s + 7]);
    return r;
}
__device__ __forceinline__ u32x4 pack8n(const f32x16& a, int s) {
    u32x4 p = pack8(a, s);
    auto x = __builtin_amdgcn_permlane32_swap(p[0], p[2], false, false);
    auto y = __builtin_amdgcn_permlane32_swap(p[1], p[3], false, false);
    u32x4 r; r[0] = x[0]; r[1] = y[0]; r[2] = x[1]; r[3] = y[1];
    return r;
}
#define ST_AUX 1
__device__ __forceinline__ void st16(char* p, u32x4 v) {
    asm volatile("global_store_dwordx4 %0, %1, off sc0 sc1\n\ts_nop 1" ::"v"(p), "v"(v) : "memory");
}
#define WAITBAR(N) do { SBAR(); asm volatile("s_waitcnt vmcnt(" #N ")\n\ts_barrier" ::: "memory"); SBAR(); } while (0)
#define BAR() do { SBAR(); asm volatile("s_barrier" ::: "memory"); SBAR(); } while (0)

__device__ __forceinline__ float bflo(unsigned u) { return __uint_as_float(u << 16); }
__device__ __forceinline__ float bfhi(unsigned u) { return __uint_as_float(u & 0xffff0000u); }

__global__ __launch_bounds__(256) void k3_out(char* __restrict__ ws, u16* __restrict__ out) {
    __shared__ f32x4 red[4][3][4][64];
    const int tid = threadIdx.x, lane = tid & 63, l31 = lane & 31, hh = lane >> 5;
    const int wid = __builtin_amdgcn_readfirstlane(tid >> 6);
    const int b = blockIdx.x, xcd = b & 7, idx = b >> 3;
    if (idx < 28) {
        const int tb = xcd * 4 + (idx & 3), nb = idx >> 2;
        const char* ap = ws + WS_OF + (size_t)(tb * 2 * 32 + wid * 8) * 1024 + lane * 16;
        const char* bp = ws + WS_WOF + (size_t)(nb * 2 * 32 + wid * 8) * 1024 + lane * 16;
        bf16x8 af[2][8], bf[2][8];
#pragma unroll
        for (int k = 0; k < 8; ++k) {
            gload16(af[0][k], ap + k * 1024); gload16(bf[0][k], bp + k * 1024);
            gload16(af[1][k], ap + 32768 + k * 1024); gload16(bf[1][k], bp + 32768 + k * 1024);
        }
        f32x16 acc[4];
#pragma unroll
        for (int i = 0; i < 4; ++i) acc[i] = f32x16{};
#define K3_STEP(k, N) do { \
            asm volatile("s_waitcnt vmcnt(" #N ")" : "+v"(af[0][k]), "+v"(bf[0][k]), "+v"(af[1][k]), "+v"(bf[1][k]), \
                         "+v"(af[0][k + 1]), "+v"(bf[0][k + 1]), "+v"(af[1][k + 1]), "+v"(bf[1][k + 1]) :: "memory"); \
            _Pragma("unroll") for (int kk = k; kk < k + 2; ++kk) \
            _Pragma("unroll") for (int i = 0; i < 4; ++i) acc[i] = MFMA(bf[i & 1][kk], af[i >> 1][kk], acc[i]); } while (0)
        K3_STEP(0, 24); K3_STEP(2, 16); K3_STEP(4, 8); K3_STEP(6, 0);
#pragma unroll
        for (int i = 0; i < 4; ++i) {
            if (i != wid) {
                const int slot = (wid - i - 1) & 3;
#pragma unroll
                for (int g = 0; g < 4; ++g)
                    red[i][slot][g][lane] = (f32x4){acc[i][4 * g], acc[i][4 * g + 1], acc[i][4 * g + 2], acc[i][4 * g + 3]};
            }
        }
        __syncthreads();
        f32x16 f = (wid == 0) ? acc[0] : (wid == 1) ? acc[1] : (wid == 2) ? acc[2] : acc[3];
#pragma unroll
        for (int s = 0; s < 3; ++s)
#pragma unroll
            for (int g = 0; g < 4; ++g) {
                const f32x4 t = red[wid][s][g][lane];
#pragma unroll
                for (int j = 0; j < 4; ++j) f[4 * g + j] += t[j];
            }
        const int t = tb * 64 + (wid >> 1) * 32 + l31, n0 = nb * 64 + (wid & 1) * 32 + 8 * hh;
        *(u32x4*)(out + (size_t)t * 896 + n0) = pack8n(f, 0);
        *(u32x4*)(out + (size_t)t * 896 + n0 + 16) = pack8n(f, 1);
    } else {
        const int task = ((idx - 28) * 8 + xcd) * 4 + wid;
        const int tt = task / 14, ng = task % 14;
        const char* yp = ws + WS_YP + ((size_t)tt * 14 + ng) * 2048 + lane * 16;
        u32x4 v[8][2];
#pragma unroll
        for (int s = 0; s < 8; ++s) {
            v[s][0] = *(const u32x4*)(yp + (size_t)s * 64 * 14 * 2048);
            v[s][1] = *(const u32x4*)(yp + (size_t)s * 64 * 14 * 2048 + 1024);
        }
        const int t = tt * 32 + l31, n0 = 448 + ng * 32 + 8 * hh;
#pragma unroll
        for (int i = 0; i < 2; ++i) {
            u32x4 o;
#pragma unroll
            for (int j = 0; j < 4; ++j) {
                float lo = 0.f, hi = 0.f;
#pragma unroll
                for (int s = 0; s < 8; ++s) { lo += bflo(v[s][i][j]); hi += bfhi(v[s][i][j]); }
                o[j] = cvtpk(lo, hi);
            }
            *(u32x4*)(out + (size_t)t * 896 + n0 + 16 * i) = o;
        }
    }
}

constexpr int K2_KV = 0, K2_B = 65536, K2_A = 122880, K2_LDS = 155648;
constexpr float ATT_T = 45.f;
constexpr float ATT_LIM = 1.1529215e18f;

__device__ __forceinline__ float bf_trunc(float x) { return __uint_as_float(__float_as_uint(x) & 0xffff0000u); }
__device__ __forceinline__ bf16x8 set_ref(bf16x8 qf1, float mref, int hh) {
    const float nm = -mref;
    const float m0 = bf_trunc(nm), r1 = nm - m0, m1 = bf_trunc(r1), m2 = bf_trunc(r1 - m1);
    u32x4 q1 = __builtin_bit_cast(u32x4, qf1);
    if (hh) {
        q1[2] = (__float_as_uint(m0) >> 16) | (__float_as_uint(m1) & 0xffff0000u);
        q1[3] = (__float_as_uint(m2) >> 16);
    }
    return __builtin_bit_cast(bf16x8, q1);
}
__device__ __forceinline__ float rowmax(const f32x16& s) {
    float mx = s[0];
#pragma unroll
    for (int r = 1; r < 16; ++r) mx = fmaxf(mx, s[r]);
    auto rr = __builtin_amdgcn_permlane32_swap(__float_as_uint(mx), __float_as_uint(mx), false, false);
    return fmaxf(__uint_as_float(rr[0]), __uint_as_float(rr[1]));
}
#define EXP4(S, i) do { S[i] = __builtin_amdgcn_exp2f(S[i]); S[i + 1] = __builtin_amdgcn_exp2f(S[i + 1]); \
                        S[i + 2] = __builtin_amdgcn_exp2f(S[i + 2]); S[i + 3] = __builtin_amdgcn_exp2f(S[i + 3]); } while (0)

__global__ __launch_bounds__(640) void k2_attn_dft(const u16* __restrict__ cre, const u16* __restrict__ cim,
                                                   char* __restrict__ ws) {
    extern __shared__ __attribute__((aligned(16))) char lds[];
    const int tid = threadIdx.x, lane = tid & 63, l31 = lane & 31, hh = lane >> 5;
    const int wid = __builtin_amdgcn_readfirstlane(tid >> 6);
    const int b = blockIdx.x, xcd = b & 7, idx = b >> 3;
    const unsigned lds0 = (unsigned)(uintptr_t)lds;
    const int h = xcd * 2 + (idx & 1);
    const int split = xcd, mt = idx >> 1, nhalf = idx & 1;
    if (wid >= 8) {
#define LD_PRIO 3
        __builtin_amdgcn_s_setprio(LD_PRIO);
        const char* bsrc = ws + WS_ABF + (size_t)(nhalf * 256 + split * 32) * 7168;
        const unsigned lo = lane * 16;
#define ROT2 0
#define SR(s) (ROT2 ? (((s) + mt) & 15) : (s))
        if (wid == 8) {
            const char* kvsrc = ws + WS_KVF + (size_t)h * 262144;
#define K2_L0(s) do { const unsigned sl = (s) & 3; \
                _Pragma("unroll") for (int i = 0; i < 4; ++i) glds4(lo, kvsrc + (size_t)(s) * 16384 + i * 4096, lds0 + K2_KV + sl * 16384 + i * 4096); \
                glds3(lo, bsrc + (size_t)SR(s) * 14336, lds0 + K2_B + sl * 14336); } while (0)
            K2_L0(0); K2_L0(1); WAITBAR(19); K2_L0(2);
            WAITBAR(19); K2_L0(3);
            for (int s = 4; s < 16; ++s) { if (!NO_BAR) WAITBAR(19);
 K2_L0(s);
 }
        } else {
            const char* cm = (const char*)((split < 4 ? cre : cim) + (size_t)(mt * 128) * SEQ + (split & 3) * 512);
            const unsigned ao = (unsigned)(lane >> 2) * 4096 + (unsigned)(((lane & 3) ^ ((lane >> 4) & 3)) * 16);
#define K2_L1(s) do { const unsigned sl = (s) & 3; \
                _Pragma("unroll") for (int i = 0; i < 8; ++i) glds(ao, cm + (size_t)i * 65536 + SR(s) * 64, lds0 + K2_A + sl * 8192 + i * 1024); \
                glds4(lo, bsrc + (size_t)SR(s) * 14336 + 3072, lds0 + K2_B + sl * 14336 + 3072); \
                glds4(lo, bsrc + (size_t)SR(s) * 14336 + 7168, lds0 + K2_B + sl * 14336 + 7168); \
                glds3(lo, bsrc + (size_t)SR(s) * 14336 + 11264, lds0 + K2_B + sl * 14336 + 11264); } while (0)
            K2_L1(0); K2_L1(1); WAITBAR(19); K2_L1(2);
            WAITBAR(19); K2_L1(3);
            for (int s = 4; s < 16; ++s) { if (!NO_BAR) WAITBAR(19);
 K2_L1(s);
 }
        }
        WAITBAR(19);
        WAITBAR(0);
        BAR();
    } else if (wid < 4) {
        const int qh = wid & 1, kh = wid >> 1;
        const int qt = (idx >> 1) * 4 + qh * 2;
        const char* qsrc = ws + WS_QF + (size_t)(h * 64 + qt) * 2048 + lane * 16;
        const bf16x8 qa0 = *(const bf16x8*)qsrc, qb0 = *(const bf16x8*)(qsrc + 2048);
        bf16x8 qa1 = *(const bf16x8*)(qsrc + 1024), qb1 = *(const bf16x8*)(qsrc + 3072);
        const f32x16 zero16 = f32x16{};
        const lds_cptr kv0 = (lds_cptr)lds + K2_KV + lane * 16;
        const lds_cptr kv = kv0 + kh * 4096;
        BAR();
        bf16x8 kA0 = ldsr128(kv0), kA1 = ldsr128(kv0 + 1024);
        f32x16 s = MFMA(kA0, qa0, zero16), sn = MFMA(kA0, qb0, zero16);
        s = MFMA(kA1, qa1, s); sn = MFMA(kA1, qb1, sn);
        kA0 = ldsr128(kv); kA1 = ldsr128(kv + 1024);
        bf16x8 vA0 = ldsr128(kv + 2048), vA1 = ldsr128(kv + 3072);
        bf16x8 kB0 = ldsr128(kv + 8192), kB1 = ldsr128(kv + 8192 + 1024), vB0, vB1;
        qa1 = set_ref(qa1, rowmax(s) + ATT_T, hh);
        qb1 = set_ref(qb1, rowmax(sn) + ATT_T, hh);
        s = MFMA(kA0, qa0, zero16);
        s = MFMA(kA1, qa1, s);
        f32x16 oa = f32x16{}, ob = f32x16{};
#define E1(S, i) S[i] = __builtin_amdgcn_exp2f(S[i])
#define UNIT(S, O, V0, V1, SN, K0, K1, Q0, Q1, L0, L1, LP) do { u32x4 p0, p1; \
            SBAR(); E1(S, 0); E1(S, 1); E1(S, 2); E1(S, 3); \
            SBAR(); SN = MFMA(K0, Q0, zero16); \
            SBAR(); E1(S, 4); E1(S, 5); p0[0] = cvtpk(S[0], S[1]); p0[1] = cvtpk(S[2], S[3]); E1(S, 6); E1(S, 7); \
            SBAR(); p0[2] = cvtpk(S[4], S[5]); p0[3] = cvtpk(S[6], S[7]); \
            SBAR(); E1(S, 8); L0 = ldsr128(LP); \
            SBAR(); O = MFMA(V0, __builtin_bit_cast(bf16x8, p0), O); \
            SBAR(); E1(S, 9); E1(S, 10); E1(S, 11); \
            SBAR(); SN = MFMA(K1, Q1, SN); \
            SBAR(); E1(S, 12); E1(S, 13); p1[0] = cvtpk(S[8], S[9]); p1[1] = cvtpk(S[10], S[11]); E1(S, 14); E1(S, 15); \
            SBAR(); p1[2] = cvtpk(S[12], S[13]); p1[3] = cvtpk(S[14], S[15]); \
            SBAR(); L1 = ldsr128(LP + 1024); \
            SBAR(); O = MFMA(V1, __builtin_bit_cast(bf16x8, p1), O); SBAR(); } while (0)
#pragma unroll 4
        for (int st = 0; st < 16; ++st) {
            const lds_cptr cur = kv + (st & 3) * 16384, nxt = kv + ((st + 1) & 3) * 16384;
            UNIT(s, oa, vA0, vA1, sn, kA0, kA1, qb0, qb1, vB0, vB1, cur + 8192 + 2048);
            if (st < 15 && (!NO_BAR || st == 0 || st > 12)) BAR();
            UNIT(sn, ob, vA0, vA1, s, kB0, kB1, qa0, qa1, kA0, kA1, nxt);
            UNIT(s, oa, vB0, vB1, sn, kB0, kB1, qb0, qb1, vA0, vA1, nxt + 2048);
            UNIT(sn, ob, vB0, vB1, s, kA0, kA1, qa0, qa1, kB0, kB1, nxt + 8192);
        }
        f32x16 o;
        {
            char* xb = lds + K2_KV + 16384 + lane * 16;
#pragma unroll
            for (int g = 0; g < 4; ++g) {
                f32x4 t;
#pragma unroll
                for (int j = 0; j < 4; ++j) t[j] = kh ? oa[4 * g + j] : ob[4 * g + j];
                *(f32x4*)(xb + (wid ^ 2) * 4096 + g * 1024) = t;
            }
            asm volatile("s_waitcnt lgkmcnt(0)\n\ts_barrier" ::: "memory");
#pragma unroll
            for (int g = 0; g < 4; ++g) {
                const f32x4 t = *(const f32x4*)(xb + wid * 4096 + g * 1024);
#pragma unroll
                for (int j = 0; j < 4; ++j) o[4 * g + j] = (kh ? ob[4 * g + j] : oa[4 * g + j]) + t[j];
            }
        }
        {
            auto rr = __builtin_amdgcn_permlane32_swap(__float_as_uint(o[0]), __float_as_uint(o[0]), false, false);
            const float l = __uint_as_float(rr[0]);
            if (__builtin_expect(__any(!(l < ATT_LIM)), 0)) {
                const char* kvg = ws + WS_KVF + (size_t)h * 262144 + lane * 16;
                const bf16x8 qf0 = kh ? qb0 : qa0;
                bf16x8 qf1 = set_ref(kh ? qb1 : qa1, 0.f, hh);
                float mref = 0.f;
                o = zero16;
                for (int t = 0; t < 64; ++t) {
                    kA0 = *(const bf16x8*)(kvg + t * 4096); kA1 = *(const bf16x8*)(kvg + t * 4096 + 1024);
                    vA0 = *(const bf16x8*)(kvg + t * 4096 + 2048); vA1 = *(const bf16x8*)(kvg + t * 4096 + 3072);
                    s = MFMA(kA0, qf0, zero16);
                    s = MFMA(kA1, qf1, s);
                    float dl = rowmax(s);
                    if (t) dl = fmaxf(dl, 0.f);
                    const float mnew = mref + dl;
                    const float d2 = mnew - mref;
                    const float alpha = t ? __builtin_amdgcn_exp2f(-d2) : 1.f;
                    mref = mnew;
                    qf1 = set_ref(qf1, mref, hh);
#pragma unroll
                    for (int r = 0; r < 16; ++r) { s[r] = __builtin_amdgcn_exp2f(s[r] - d2); o[r] *= alpha; }
                    u32x4 p0 = pack8(s, 0), p1 = pack8(s, 1);
                    o = MFMA(vA0, __builtin_bit_cast(bf16x8, p0), o);
                    o = MFMA(vA1, __builtin_bit_cast(bf16x8, p1), o);
                }
            }
        }
        auto rr = __builtin_amdgcn_permlane32_swap(__float_as_uint(o[0]), __float_as_uint(o[0]), false, false);
        const float linv = 1.f / __uint_as_float(rr[0]);
#pragma unroll
        for (int r = 0; r < 16; ++r) o[r] *= linv;
        char* oo = ws + WS_OF + (size_t)((qt + kh) * 32 + h * 2) * 1024 + lane * 16;
        st16(oo, pack8(o, 0));
        st16(oo + 1024, pack8(o, 1));
    } else {
        const int w = wid - 4;
        f32x16 acc[7];
#pragma unroll
        for (int n = 0; n < 7; ++n) acc[n] = f32x16{};
        const int asw = (l31 >> 2) & 3;
        const lds_cptr a0 = (lds_cptr)lds + K2_A + (w * 32 + l31) * 64 + ((hh ^ asw) * 16);
        const lds_cptr a1 = (lds_cptr)lds + K2_A + (w * 32 + l31) * 64 + (((2 + hh) ^ asw) * 16);
        const lds_cptr bb = (lds_cptr)lds + K2_B + lane * 16;
        BAR();
        bf16x8 bq[7], af, an;
        af = ldsr128(a0);
#pragma unroll
        for (int n = 0; n < 7; ++n) bq[n] = ldsr128(bb + n * 1024);
        for (int st = 0; st < 16; ++st) {
            const int sl = st & 3, sn = (st + 1) & 3;
            const lds_cptr bc = bb + sl * 14336 + 7168, bn = bb + sn * 14336;
            an = ldsr128(a1 + sl * 8192);
#pragma unroll
            for (int n = 0; n < GEMM_N; ++n) {
                SBAR();
                acc[n] = MFMA(bq[n], af, acc[n]);
                bq[n] = ldsr128(bc + n * 1024);
            }
            if (st < 15 && (!NO_BAR || st == 0 || st > 12)) BAR();
            af = ldsr128(a0 + sn * 8192);
#pragma unroll
            for (int n = 0; n < GEMM_N; ++n) {
                SBAR();
                acc[n] = MFMA(bq[n], an, acc[n]);
                bq[n] = ldsr128(bn + n * 1024);
            }
        }
        char* yo = ws + WS_YP + ((size_t)(split * 64 + mt * 4 + w) * 14 + nhalf * 7) * 2048 + lane * 16;
#pragma unroll
        for (int n = 0; n < 7; ++n) {
            st16(yo + n * 2048, pack8n(acc[n], 0));
            st16(yo + n * 2048 + 1024, pack8n(acc[n], 1));
        }
        BAR();
    }
}

constexpr int K1_SLOT = 40960, K1_LDS = 4 * K1_SLOT;

#define K1_LOADER(DMA) do { \
        DMA(0); DMA(1); WAITBAR(20); DMA(2); DMA(3); WAITBAR(40); \
        WAITBAR(20); DMA(4); WAITBAR(20); DMA(5); WAITBAR(20); DMA(6); WAITBAR(20); WAITBAR(0); } while (0)

__global__ __launch_bounds__(640) void k1_proj(const u16* __restrict__ xA, const u16* __restrict__ wqkv,
                                               const u16* __restrict__ wo, const u16* __restrict__ xB,
                                               const u16* __restrict__ dre, const u16* __restrict__ dim,
                                               const u16* __restrict__ cre, const u16* __restrict__ cim,
                                               char* __restrict__ ws) {
    extern __shared__ __attribute__((aligned(16))) char lds[];
    const int tid = threadIdx.x, lane = tid & 63, l31 = lane & 31, hh = lane >> 5;
    const int wid = __builtin_amdgcn_readfirstlane(tid >> 6);
    const int b = blockIdx.x, xcd = b & 7, idx = b >> 3;
    const unsigned lds0 = (unsigned)(uintptr_t)lds;
    const unsigned xoff0 = (unsigned)(lane >> 3) * (DM * 2) + (unsigned)(((lane & 7) ^ (lane >> 4)) * 16);
    const unsigned xoff1 = (unsigned)(lane >> 3) * (DM * 2) + (unsigned)(((lane & 7) ^ (4 + (lane >> 4))) * 16) + 8 * DM * 2;
    const int sw = (l31 >> 1) & 7;
    const int ld = wid - 8;
#define ROTX 1
#define ROT1 1
    const int rot = (idx * ROT1 + xcd * ROTX) % 7;
#define CC(c) ((c) + rot >= 7 ? (c) + rot - 7 : (c) + rot)
    if (idx < 16) {
        const int hp = (xcd & 1) * 4 + (idx & 3), mb = (xcd >> 1) * 4 + (idx >> 2);
        if (wid >= 8) {
            __builtin_amdgcn_s_setprio(3);
            const char* xs = (const char*)(xA + (size_t)(mb * 128 + ld * 64) * DM);
            const char* wsrc = (const char*)(wqkv + (size_t)(ld * 32) * 1344 + hp * 56);
            int wc = (lane & 7) ^ (((lane >> 4) & 1) * 4); wc = wc > 6 ? 6 : wc;
            const unsigned woff = (unsigned)(lane >> 3) * 2688 + (unsigned)wc * 16;
#define K1A_DMA(c) do { int cc_ = CC(c); unsigned sl = lds0 + ((c) & 3) * K1_SLOT; asm volatile("" : "+s"(cc_), "+s"(sl)); \
                _Pragma("unroll") for (int i = 0; i < 4; ++i) { \
                    glds(xoff0, xs + i * 16 * DM * 2 + cc_ * 128, sl + ld * 8192 + i * 2048); \
                    glds(xoff1, xs + i * 16 * DM * 2 + cc_ * 128, sl + ld * 8192 + i * 2048 + 1024); \
                    _Pragma("unroll") for (int g = 0; g < 3; ++g) \
                        glds(woff, wsrc + (size_t)(cc_ * 64 + i * 8) * 2688 + g * 896, sl + 16384 + g * 8192 + ld * 4096 + i * 1024); } } while (0)
            K1_LOADER(K1A_DMA);
            return;
        }
        const int tg = wid & 3, hs = wid >> 2, h = hp * 2 + hs;
        f32x16 acc[3];
#pragma unroll
        for (int g = 0; g < 3; ++g) acc[g] = f32x16{};
        const int wrow = 8 * hh + ((lane & 15) >> 2), wrot = ((lane >> 3) & 1) * 64, wcol = ((lane >> 4) & 1) * 32 + (lane & 3) * 8;
        const lds_cptr wqk = (lds_cptr)lds + 16384 + wrow * 128 + ((hs * 56 + wcol + wrot) & 127);
        const lds_cptr wv = (lds_cptr)lds + 16384 + 16384 + wrow * 128 + ((hs * 56 + 120 + wcol + wrot) & 127);
        const lds_cptr xr = (lds_cptr)lds + (tg * 32 + l31) * 128;
        bf16x8 xf[2], wf[2][3];
#define K1A_RD(ks) do { const int so = (((ks) >> 2) & 3) * K1_SLOT, kk = (ks) & 3; \
            xf[(ks) & 1] = ldsr128(xr + so + (((kk * 2 + hh) ^ sw) * 16)); \
            _Pragma("unroll") for (int g = 0; g < 3; ++g) { \
                lds_cptr p = (g < 2 ? wqk + g * 8192 : wv) + so + kk * 2048; \
                s16x4 lo = vtr(p), hi = vtr(p + 512); \
                wf[(ks) & 1][g] = (bf16x8){lo[0], lo[1], lo[2], lo[3], hi[0], hi[1], hi[2], hi[3]}; } } while (0)
        BAR();
        K1A_RD(0);
#pragma unroll
        for (int ks = 0; ks < 28; ++ks) {
            if ((ks & 3) == 3 && ks < 27) BAR();
            if (ks < 27) K1A_RD(ks + 1);
            SBAR();
            acc[0] = MFMA(wf[ks & 1][0], xf[ks & 1], acc[0]);
            acc[1] = MFMA(wf[ks & 1][1], xf[ks & 1], acc[1]);
            acc[2] = MFMA(xf[ks & 1], wf[ks & 1][2], acc[2]);
            SBAR();
        }
        const float qs = 0.18898223650461362f * 1.4426950408889634f;
        const int tt = mb * 4 + tg;
#pragma unroll
        for (int r = 0; r < 16; ++r) acc[0][r] *= qs;
        if (hh) {
#pragma unroll
            for (int r = 12; r < 16; ++r) { acc[0][r] = 0.f; acc[1][r] = (r < 15) ? 1.f : 0.f; }
        }
        if (l31 < 4) {
#pragma unroll
            for (int r = 0; r < 16; ++r) acc[2][r] = (l31 == 0) ? 1.f : 0.f;
        }
        char* qo = ws + WS_QF + (size_t)(h * 64 + tt) * 2048 + lane * 16;
        char* kvo = ws + WS_KVF + (size_t)(h * 64 + tt) * 4096 + lane * 16;
        st16(qo, pack8(acc[0], 0)); st16(qo + 1024, pack8(acc[0], 1));
        st16(kvo, pack8(acc[1], 0)); st16(kvo + 1024, pack8(acc[1], 1));
        st16(kvo + 2048, pack8(acc[2], 0)); st16(kvo + 3072, pack8(acc[2], 1));
    } else if (idx < 30) {
        const int j = idx - 16, im = xcd >> 2;
        const int sb = (xcd & 3) * 2 + (j & 1), et = j >> 1;
        if (wid >= 8) {
            __builtin_amdgcn_s_setprio(3);
            const char* xs = (const char*)(xB + (size_t)(sb * 256 + ld * 128) * DM);
            const char* dsrc = (const char*)((im ? dim : dre) + (size_t)(et * 64 + ld * 32) * DM);
#define K1B_DMA(c) do { int cc_ = CC(c); unsigned sl = lds0 + ((c) & 3) * K1_SLOT; asm volatile("" : "+s"(cc_), "+s"(sl)); \
                _Pragma("unroll") for (int i = 0; i < 2; ++i) { \
                    glds(xoff0, dsrc + i * 16 * DM * 2 + cc_ * 128, sl + 32768 + ld * 4096 + i * 2048); \
                    glds(xoff1, dsrc + i * 16 * DM * 2 + cc_ * 128, sl + 32768 + ld * 4096 + i * 2048 + 1024); } \
                _Pragma("unroll") for (int i = 0; i < 8; ++i) { \
                    glds(xoff0, xs + i * 16 * DM * 2 + cc_ * 128, sl + ld * 16384 + i * 2048); \
                    glds(xoff1, xs + i * 16 * DM * 2 + cc_ * 128, sl + ld * 16384 + i * 2048 + 1024); } } while (0)
            K1_LOADER(K1B_DMA);
#define PF_A 2
            {
                unsigned dummy = 0;
#pragma unroll
                for (int i = 0; i < 2; ++i) {
                    const int q = ld * 64 + lane + i * 128, r = j * 147 + q;
                    const char* a = (const char*)((xcd < 4 ? cre : cim) + (size_t)r * SEQ + (xcd & 3) * 512);
                    if (q < 147 && r < SEQ) {
#pragma unroll
                        for (int l = 0; l < PF_A; ++l)
                            asm volatile("global_load_dword %0, %1, off" : "+v"(dummy) : "v"(a + l * 128) : "memory");
                    }
                }
                asm volatile("s_waitcnt vmcnt(0)" : "+v"(dummy) :: "memory");
            }
            return;
        }
        f32x16 acc[2];
        acc[0] = f32x16{}; acc[1] = f32x16{};
        const lds_cptr xr = (lds_cptr)lds + (wid * 32 + l31) * 128;
        const lds_cptr dr = (lds_cptr)lds + 32768 + l31 * 128;
        bf16x8 xf[2], df[2][2];
#define K1B_RD(ks) do { const int so = (((ks) >> 2) & 3) * K1_SLOT, co = ((((ks) & 3) * 2 + hh) ^ sw) * 16; \
            xf[(ks) & 1] = ldsr128(xr + so + co); df[(ks) & 1][0] = ldsr128(dr + so + co); df[(ks) & 1][1] = ldsr128(dr + so + 4096 + co); } while (0)
        BAR();
        K1B_RD(0);
#pragma unroll
        for (int ks = 0; ks < 28; ++ks) {
            if ((ks & 3) == 3 && ks < 27) BAR();
            if (ks < 27) K1B_RD(ks + 1);
            SBAR();
            acc[0] = MFMA(xf[ks & 1], df[ks & 1][0], acc[0]);
            acc[1] = MFMA(xf[ks & 1], df[ks & 1][1], acc[1]);
            SBAR();
        }
        const float sg = im ? -1.f : 1.f;
        const int ks0 = im * 128 + sb * 16 + wid * 2;
#pragma unroll
        for (int nt = 0; nt < 2; ++nt) {
            const int ng = et * 2 + nt, nhalf = ng / 7, n7 = ng % 7;
#pragma unroll
            for (int r = 0; r < 16; ++r) acc[nt][r] *= sg;
#pragma unroll
            for (int s = 0; s < 2; ++s)
                st16(ws + WS_ABF + ((size_t)(nhalf * 256 + ks0 + s) * 7 + n7) * 1024 + lane * 16, pack8n(acc[nt], s));
        }
    } else if (wid < 8) {
        const int w0 = (xcd * 2 + (idx - 30)) * 8 + wid;
        u16 v[4][8];
#pragma unroll
        for (int i = 0; i < 4; ++i) {
            const int f = min(w0 + i * 128, 447);
            const int nt = f >> 5, ks = f & 31, h = ks >> 1, s = ks & 1;
#pragma unroll
            for (int j = 0; j < 8; ++j) {
                const int rho = 16 * s + 8 * (j >> 2) + 4 * hh + (j & 3);
                const u16 t = wo[(size_t)(h * 28 + max(rho - 4, 0)) * DM + nt * 32 + l31];
                v[i][j] = (rho >= 4) ? t : (u16)0;
            }
        }
#pragma unroll
        for (int i = 0; i < 4; ++i) {
            const int f = min(w0 + i * 128, 447);
            u32x4 o;
#pragma unroll
            for (int j = 0; j < 4; ++j) o[j] = (unsigned)v[i][2 * j] | ((unsigned)v[i][2 * j + 1] << 16);
            *(u32x4*)(ws + WS_WOF + (size_t)f * 1024 + lane * 16) = o;
        }
    } else if (idx == 30) {
#define PF_CODE 1
        unsigned long long c0, c1;
        asm volatile("s_getpc_b64 s[40:41]\n\ts_add_u32 s40, s40, _Z6k3_outPcPt@rel32@lo+4\n\ts_addc_u32 s41, s41, _Z6k3_outPcPt@rel32@hi+12\n\ts_mov_b64 %0, s[40:41]" : "=s"(c0) :: "scc", "s40", "s41");
        asm volatile("s_getpc_b64 s[40:41]\n\ts_add_u32 s40, s40, _Z7k1_projPKtS0_S0_S0_S0_S0_S0_S0_Pc@rel32@lo+4\n\ts_addc_u32 s41, s41, _Z7k1_projPKtS0_S0_S0_S0_S0_S0_S0_Pc@rel32@hi+12\n\ts_mov_b64 %0, s[40:41]" : "=s"(c1) :: "scc", "s40", "s41");
        unsigned dummy = 0;
#pragma unroll
        for (int i = 0; i < 5; ++i) {
            const unsigned long long a = c0 + (unsigned)((i * 128 + ld * 64 + lane) * 64);
            if (a < c1) asm volatile("global_load_dword %0, %1, off" : "+v"(dummy) : "v"(a) : "memory");
        }
        asm volatile("s_waitcnt vmcnt(0)" : "+v"(dummy) :: "memory");
    }
}

extern "C" void kernel_launch(void* const* d_in, const int* in_sizes, int n_in, void* d_out, int out_size, void* d_ws,
                              size_t ws_size, hipStream_t stream) {
    const u16* xA = (const u16*)d_in[0];
    const u16* wqkv = (const u16*)d_in[1];
    const u16* wo = (const u16*)d_in[2];
    const u16* xB = (const u16*)d_in[3];
    const u16* cre = (const u16*)d_in[4];
    const u16* cim = (const u16*)d_in[5];
    const u16* dre = (const u16*)d_in[6];
    const u16* dim = (const u16*)d_in[7];
    char* ws = (char*)d_ws;
    (void)hipFuncSetAttribute((const void*)k1_proj, hipFuncAttributeMaxDynamicSharedMemorySize, K1_LDS);
    (void)hipFuncSetAttribute((const void*)k2_attn_dft, hipFuncAttributeMaxDynamicSharedMemorySize, K2_LDS);
    k1_proj<<<256, 640, K1_LDS, stream>>>(xA, wqkv, wo, xB, dre, dim, cre, cim, ws);
    k2_attn_dft<<<256, 640, K2_LDS, stream>>>(cre, cim, ws);
    k3_out<<<448, 256, 0, stream>>>(ws, (u16*)d_out);
}
```
